```python
import torch

def _softmax(x, axis=-1):
    return torch.softmax(x, dim=axis)
def _gelu(x):
    return torch.nn.functional.gelu(x, approximate="tanh")
def _silu(x):
    return torch.nn.functional.silu(x)
def _sigmoid(x):
    return torch.sigmoid(x)
def _layernorm(x, axis=-1, eps=1e-5):
    m = x.mean(dim=axis, keepdim=True)
    v = x.var(dim=axis, keepdim=True, unbiased=False)
    return (x - m) / torch.sqrt(v + eps)
def _scatter_add(tgt, idx, src, axis=0):
    return tgt.clone().index_add_(axis, idx, src)
def _segment_sum(src, seg, n):
    out = torch.zeros((n,) + src.shape[1:], dtype=src.dtype)
    return out.index_add_(0, seg, src)
def _pad(x, pw):
    flat = tuple(v for pair in reversed(pw) for v in pair)
    return torch.nn.functional.pad(x, flat)

kernel_name = 'recurrent_scan__ssm_conv_s5__dense_activation_silu__prompt'

def setup_inputs():

    _w = (lambda a: (lambda t: t.to(torch.bfloat16) if t.is_floating_point() else t)(torch.as_tensor(a)))
    def _A_setup():
        B, S, D = 1, 2048, 512
        P = D
        Lambda_re = -0.5 * (lambda _x: torch.exp(torch.as_tensor(_x)))((lambda *s: torch.randn(*s, dtype=torch.bfloat16) * 0.8)(P))
        Lambda_im = (lambda *s: torch.randn(*s, dtype=torch.bfloat16) * 0.8)(P) * 1.0
        B_re = (lambda *s: torch.randn(*s, dtype=torch.bfloat16) * 0.8)(P, D) * (D ** -0.5)
        B_im = (lambda *s: torch.randn(*s, dtype=torch.bfloat16) * 0.8)(P, D) * (D ** -0.5)
        C_re = (lambda *s: torch.randn(*s, dtype=torch.bfloat16) * 0.8)(D, P) * (P ** -0.5)
        C_im = (lambda *s: torch.randn(*s, dtype=torch.bfloat16) * 0.8)(D, P) * (P ** -0.5)
        log_dt = (lambda *s: torch.randn(*s, dtype=torch.bfloat16) * 0.8)(P) * 0.3 - 2.0
        Dskip = (lambda *s: torch.randn(*s, dtype=torch.bfloat16) * 0.8)(D)
        return {
            "x": _w((lambda *s: torch.randn(*s, dtype=torch.bfloat16) * 0.05)(B, S, D)),
            "Lambda_re": _w(Lambda_re),
            "Lambda_im": _w(Lambda_im),
            "B_re": _w(B_re),
            "B_im": _w(B_im),
            "C_re": _w(C_re),
            "C_im": _w(C_im),
            "log_dt": _w(log_dt),
            "Dskip": _w(Dskip),
        }
    _A_in = _A_setup()

    def _B_setup():
        B, S, D, F = 1, 2048, 512, 1792
        return {
            "x": _w((lambda *s: torch.randn(*s, dtype=torch.bfloat16) * 0.05)(B, S, D)),
            "w1": _w((lambda *s: torch.randn(*s, dtype=torch.bfloat16) * 0.8)(D, F) * (D ** -0.5)),
            "w2": _w((lambda *s: torch.randn(*s, dtype=torch.bfloat16) * 0.2)(F, D) * (F ** -0.5)),
        }
    _B_in = _B_setup()

    return {
        "A_x": _A_in["x"],
        "A_Lambda_re": _A_in["Lambda_re"],
        "A_Lambda_im": _A_in["Lambda_im"],
        "A_B_re": _A_in["B_re"],
        "A_B_im": _A_in["B_im"],
        "A_C_re": _A_in["C_re"],
        "A_C_im": _A_in["C_im"],
        "A_log_dt": _A_in["log_dt"],
        "A_Dskip": _A_in["Dskip"],
        "B_x": _B_in["x"],
        "B_w1": _B_in["w1"],
        "B_w2": _B_in["w2"],
    }

def reference(A_x, A_Lambda_re, A_Lambda_im, A_B_re, A_B_im, A_C_re, A_C_im, A_log_dt, A_Dskip, B_x, B_w1, B_w2):
    def _A_ref(x, Lambda_re, Lambda_im, B_re, B_im, C_re, C_im, log_dt, Dskip):
        Bsz, L, H = x.shape
        Lam = Lambda_re + 1j * Lambda_im
        Bmat = B_re + 1j * B_im
        Cmat = C_re + 1j * C_im
        dt = torch.exp(log_dt)
        Lam_bar = torch.exp(dt * Lam)
        B_bar = ((Lam_bar - 1.0) / Lam)[:, None] * Bmat
        Bu = torch.einsum("bld,pd->blp", x + 0j, B_bar)
        h = torch.zeros_like(Bu[:, 0, :])
        hs = []
        for t in range(L):
            h = Lam_bar[None, :] * h + Bu[:, t, :]
            hs.append(h)
        hseq = torch.stack(hs, 1)
        y = 2.0 * torch.einsum("blp,dp->bld", hseq, Cmat).real + x * Dskip[None, None, :]
        return y

    def _B_ref(x, w1, w2):
        h = torch.einsum("bsd,df->bsf", x, w1)
        h = _silu(h)
        out = torch.einsum("bsf,fd->bsd", h, w2)
        return out

    x = A_x
    xn = _layernorm(x, axis=-1)
    s = _A_ref(xn, A_Lambda_re, A_Lambda_im, A_B_re, A_B_im, A_C_re, A_C_im, A_log_dt, A_Dskip)
    g = _silu(_B_ref(xn, B_w1, B_w2))
    return x + s * g
```

```cpp
#include <hip/hip_runtime.h>
#include <hip/hip_bf16.h>
#include <stdint.h>

typedef unsigned short u16;
__device__ __forceinline__ float bf2f(u16 b){ return __builtin_bit_cast(float, (unsigned)b << 16); }
__device__ __forceinline__ u16 f2bf(float f){ __hip_bfloat16 h = __float2bfloat16(f); return __builtin_bit_cast(u16, h); }
__device__ __forceinline__ float rbf(float f){ return bf2f(f2bf(f)); }

constexpr int S=2048, D=512, P=512, F=1792;

__global__ void k_ln(const u16* x, u16* xn){
  int row = blockIdx.x; int t = threadIdx.x;
  float v[8]; float s=0;
  for(int j=0;j<8;j++){ v[j]=bf2f(x[row*D + t*8+j]); s+=v[j]; }
  for(int o=32;o>0;o>>=1) s+=__shfl_xor(s,o);
  float m = s/D; float q=0;
  for(int j=0;j<8;j++){ float d=v[j]-m; q+=d*d; }
  for(int o=32;o>0;o>>=1) q+=__shfl_xor(q,o);
  float mb = rbf(m); float vb = rbf(q/D);
  float sd = rbf(sqrtf(rbf(vb+1e-5f)));
  for(int j=0;j<8;j++){ xn[row*D+t*8+j] = f2bf(rbf(v[j]-mb)/sd); }
}
template<bool BNK, int MODE>
__global__ void k_gemm(const u16* A, const u16* B, float* Cf, u16* Cb, int M, int N, int K, int lda){
  int n = blockIdx.x*64+threadIdx.x; int m = blockIdx.y;
  float acc=0;
  for(int k=0;k<K;k++){ float b = BNK? bf2f(B[(size_t)n*K+k]) : bf2f(B[(size_t)k*N+n]); acc += bf2f(A[(size_t)m*lda+k])*b; }
  if(MODE==0) Cf[(size_t)m*N+n]=acc;
  else if(MODE==1){ float h=rbf(acc); Cb[(size_t)m*N+n]=f2bf(h/(1.f+expf(-h))); }
}
__global__ void k_scan(const float* Ur, const float* Ui, const u16* Lre, const u16* Lim, const u16* logdt, float* Hr, float* Hi){
  int p = blockIdx.x*64+threadIdx.x;
  float lr=bf2f(Lre[p]), li=bf2f(Lim[p]); float dt=rbf(expf(bf2f(logdt[p])));
  float zr=dt*lr, zi=dt*li; float e=expf(zr); float br=e*cosf(zi), bi=e*sinf(zi);
  float nr=br-1.f, ni=bi; float den=lr*lr+li*li;
  float cr=(nr*lr+ni*li)/den, ci=(ni*lr-nr*li)/den;
  float hr=0,hi=0;
  for(int t=0;t<S;t++){
    float ur=Ur[t*P+p], ui=Ui[t*P+p];
    float bur=cr*ur-ci*ui, bui=cr*ui+ci*ur;
    float nhr=br*hr-bi*hi+bur, nhi=br*hi+bi*hr+bui;
    hr=nhr; hi=nhi; Hr[t*P+p]=hr; Hi[t*P+p]=hi;
  }
}
__global__ void k_final(const u16* x, const u16* xn, const float* Hr, const float* Hi, const u16* Cre, const u16* Cim, const u16* Dsk, const u16* g, float* out){
  int d = blockIdx.x*64+threadIdx.x; int t = blockIdx.y;
  float acc=0;
  for(int p=0;p<P;p++){ acc += Hr[t*P+p]*bf2f(Cre[d*P+p]) - Hi[t*P+p]*bf2f(Cim[d*P+p]); }
  float s = 2.f*acc + rbf(bf2f(xn[t*D+d])*bf2f(Dsk[d]));
  out[t*D+d] = bf2f(x[t*D+d]) + s*bf2f(g[t*D+d]);
}

extern "C" void kernel_launch(void* const* d_in, const int* in_sizes, int n_in,
                              void* d_out, int out_size, void* d_ws, size_t ws_size,
                              hipStream_t stream) {
  const u16* x=(const u16*)d_in[0]; const u16* Lre=(const u16*)d_in[1]; const u16* Lim=(const u16*)d_in[2];
  const u16* Bre=(const u16*)d_in[3]; const u16* Bim=(const u16*)d_in[4]; const u16* Cre=(const u16*)d_in[5]; const u16* Cim=(const u16*)d_in[6];
  const u16* logdt=(const u16*)d_in[7]; const u16* Dsk=(const u16*)d_in[8]; const u16* w1=(const u16*)d_in[10]; const u16* w2=(const u16*)d_in[11];
  char* ws=(char*)d_ws;
  u16* xn=(u16*)ws; ws+=S*D*2;
  float* Ur=(float*)ws; ws+=S*P*4; float* Ui=(float*)ws; ws+=S*P*4;
  float* Hr=(float*)ws; ws+=S*P*4; float* Hi=(float*)ws; ws+=S*P*4;
  u16* h1=(u16*)ws; ws+=S*F*2; u16* g=(u16*)ws; ws+=S*D*2;
  k_ln<<<S,64,0,stream>>>(x,xn);
  k_gemm<true,0><<<dim3(P/64,S),64,0,stream>>>(xn,Bre,Ur,nullptr,S,P,D,D);
  k_gemm<true,0><<<dim3(P/64,S),64,0,stream>>>(xn,Bim,Ui,nullptr,S,P,D,D);
  k_scan<<<P/64,64,0,stream>>>(Ur,Ui,Lre,Lim,logdt,Hr,Hi);
  k_gemm<false,1><<<dim3(F/64,S),64,0,stream>>>(xn,w1,nullptr,h1,S,F,D,D);
  k_gemm<false,1><<<dim3(D/64,S),64,0,stream>>>(h1,w2,nullptr,g,S,D,F,F);
  k_final<<<dim3(D/64,S),64,0,stream>>>(x,xn,Hr,Hi,Cre,Cim,Dsk,g,(float*)d_out);
}
```

```cpp
#include <hip/hip_runtime.h>
#include <hip/hip_bf16.h>
#include <stdint.h>

typedef unsigned short u16;
typedef unsigned v4u __attribute__((ext_vector_type(4)));
typedef unsigned v2u __attribute__((ext_vector_type(2)));
typedef float f32x4 __attribute__((ext_vector_type(4)));
typedef float f32x2 __attribute__((ext_vector_type(2)));
typedef short bf16x8 __attribute__((ext_vector_type(8)));
typedef short v4s __attribute__((ext_vector_type(4)));
typedef __bf16 v2bf __attribute__((ext_vector_type(2)));
#define LAS __attribute__((address_space(3)))
#define GAS __attribute__((address_space(1)))
#define RLX_AGENT __ATOMIC_RELAXED, __HIP_MEMORY_SCOPE_AGENT

namespace {
constexpr int S_ = 2048, D_ = 512, F_ = 1792;
constexpr int KS2 = 88;
constexpr size_t WS_H = 0;
constexpr size_t WS_H_BYTES = (size_t)32 * KS2 * 4096;
constexpr size_t WS_ST = WS_H + WS_H_BYTES;
constexpr size_t WS_E = WS_ST + 2048 * 8;
constexpr size_t WS_EP = WS_E + 16 * 512 * 16;
constexpr size_t WS_END = WS_EP + 64 * 128;
constexpr int STAGE = 40960, OFF_S = 16384, OFF_W = 24576;
constexpr int LDS_BYTES = 163840;
constexpr unsigned SPIN_CAP = 1u << 16;

__device__ __forceinline__ float bf2f(unsigned b) { return __builtin_bit_cast(float, b << 16); }
__device__ __forceinline__ unsigned pkbf(float lo, float hi) {
  v2bf v; v.x = (__bf16)lo; v.y = (__bf16)hi; return __builtin_bit_cast(unsigned, v);
}
__device__ __forceinline__ float rbf(float f) { return bf2f(pkbf(f, 0.f) & 0xffffu); }
__device__ __forceinline__ f32x4 mfma(v4u a, v4u b, f32x4 c) {
  return __builtin_amdgcn_mfma_f32_16x16x32_bf16(__builtin_bit_cast(bf16x8, a), __builtin_bit_cast(bf16x8, b), c, 0, 0, 0);
}
__device__ __forceinline__ float dot2(unsigned a, unsigned b, float c) {
  return __builtin_amdgcn_fdot2_f32_bf16(__builtin_bit_cast(v2bf, a), __builtin_bit_cast(v2bf, b), c, false);
}
__device__ __forceinline__ void glds16(const void* g, LAS char* l) {
  __builtin_amdgcn_global_load_lds((const GAS void*)g, (LAS void*)l, 16, 0, 0);
}
__device__ __forceinline__ v2u trread(LAS char* p) {
  return __builtin_bit_cast(v2u, __builtin_amdgcn_ds_read_tr16_b64_v4i16((LAS v4s*)p));
}
__device__ __forceinline__ float silu_f(float h) { return h * __builtin_amdgcn_rcpf(1.f + __expf(-h)); }
struct cplx { float r, i; };
__device__ __forceinline__ cplx cmul(cplx a, cplx b) { return {a.r * b.r - a.i * b.i, a.r * b.i + a.i * b.r}; }
__device__ __forceinline__ cplx cfma(cplx a, cplx b, cplx c) {
  return {__builtin_fmaf(a.r, b.r, __builtin_fmaf(-a.i, b.i, c.r)), __builtin_fmaf(a.r, b.i, __builtin_fmaf(a.i, b.r, c.i))};
}
__device__ __forceinline__ unsigned hashA(unsigned a, unsigned b) { unsigned h = a * 0x9E3779B1u + (b ^ (b >> 15)) * 0x85EBCA77u; return h ^ (h >> 13); }
__device__ __forceinline__ unsigned hashB(unsigned a, unsigned b) { unsigned h = b * 0xC2B2AE3Du + (a ^ (a >> 16)) * 0x27D4EB2Fu; return h ^ (h >> 15); }
#define WAITV(n) asm volatile("s_waitcnt vmcnt(" #n ")" ::: "memory")
#define BAR() __builtin_amdgcn_s_barrier()
#define ST_DECL
#define ST(i)
#define ST_FLUSH(kid, wsp)

template <int UG>
__device__ __forceinline__ void p1_body(const u16* __restrict__ x, const u16* __restrict__ Lre, const u16* __restrict__ Lim,
                                        const u16* __restrict__ Bre, const u16* __restrict__ Bim, const u16* __restrict__ logdt,
                                        const u16* __restrict__ w1, char* __restrict__ ws, LAS char* L, int r, int c, int w) {
  constexpr int NU = UG == 0 ? 6 : 5;
  constexpr int NF = UG == 0 ? 2 : 5;
  constexpr int V0 = UG == 0 ? 0 : 2;
  const int lane = threadIdx.x & 63, tok = lane & 15, g = lane >> 4, tg = w & 3;
  const int codd = c & 1;
  ST_DECL ST(0);
  const u16 *sx0, *sx1, *ss, *sw0, *sw1;
  {
    const int r8 = lane >> 3, p8 = lane & 7;
    const int row0 = 16 * w + r8, row1 = row0 + 8;
    sx0 = x + (size_t)(128 * r + row0) * D_ + ((p8 ^ ((row0 >> 1) & 7)) << 3);
    sx1 = x + (size_t)(128 * r + row1) * D_ + ((p8 ^ ((row1 >> 1) & 7)) << 3);
    const int n = 8 * w + r8;
    ss = ((n & 1) ? Bim : Bre) + (size_t)(32 * c + (n >> 1)) * D_ + ((p8 ^ ((n >> 1) & 7)) << 3);
    const int pos = lane & 15;
    const int k0 = 8 * w + g, k1 = k0 + 4;
    const int f0 = (k0 & 3) | (((k0 >> 3) & 1) << 2), f1 = (k1 & 3) | (((k1 >> 3) & 1) << 2);
    const int colbase = 112 * c - 16 * codd;
    sw0 = w1 + (size_t)k0 * F_ + colbase + (((((pos >> 1) ^ f0) << 1) | (pos & 1)) << 3);
    sw1 = w1 + (size_t)k1 * F_ + colbase + (((((pos >> 1) ^ f1) << 1) | (pos & 1)) << 3);
  }
  auto issue = [&](int s) {
    LAS char* sb = L + (s & 3) * STAGE;
    glds16(sx0 + s * 64, sb + (2 * w) * 1024);
    glds16(sx1 + s * 64, sb + (2 * w + 1) * 1024);
    glds16(ss + s * 64, sb + OFF_S + w * 1024);
    glds16(sw0 + (size_t)s * 64 * F_, sb + OFF_W + (2 * w) * 1024);
    glds16(sw1 + (size_t)s * 64 * F_, sb + OFF_W + (2 * w + 1) * 1024);
  };
  const int sch = lane & 7, seg = lane >> 3, chg = 32 * c + 8 * tg + sch;
  unsigned plre = 0, plim = 0, pldt = 0; unsigned long long ep = 0;
  if (UG == 0) {
    plre = Lre[chg]; plim = Lim[chg]; pldt = logdt[chg];
    ep = __hip_atomic_load((GAS unsigned long long*)(ws + WS_EP + (size_t)(c * 4 + tg) * 128), RLX_AGENT) + 1ull;
  }
  issue(0); issue(1); issue(2); issue(3);
  const int swz = (tok >> 1) & 7;
  const int xoff = (32 * tg + tok) * 128 + ((g ^ swz) << 4);
  const int soff = OFF_S + tok * 128 + ((g ^ swz) << 4);
  const int q = tok >> 2, p4 = lane & 3, fl = q | ((g & 1) << 2);
  int woff[NF];
#pragma unroll
  for (int i = 0; i < NF; i++) {
    const int v = V0 + i;
    const int pi = codd ? (v == 6 ? 1 : v + 2) : v;
    woff[i] = OFF_W + (8 * g + q) * 256 + ((pi ^ fl) << 5) + p4 * 8;
  }
  f32x4 acc[NU][2];
#pragma unroll
  for (int u = 0; u < NU; u++) { acc[u][0] = f32x4{0, 0, 0, 0}; acc[u][1] = f32x4{0, 0, 0, 0}; }
  float cs[NU];
#pragma unroll
  for (int u = 0; u < NU; u++) cs[u] = 0.f;
  float sm[2] = {0.f, 0.f}, sq[2] = {0.f, 0.f};
  constexpr unsigned ONES = 0x3f803f80u;

#pragma unroll
  for (int s = 0; s < 8; s++) {
    if (s == 0) WAITV(15); else if (s <= 5) WAITV(10); else if (s == 6) WAITV(5); else WAITV(0);
    BAR();
    if (s == 0) ST(1); if (s == 4) ST(2); if (s == 7) ST(3);
    if (s >= 1 && s + 3 < 8) issue(s + 3);
    LAS char* sb = L + (s & 3) * STAGE;
#pragma unroll
    for (int kk = 0; kk < 2; kk++) {
      v4u xf[2], wf[NU];
#pragma unroll
      for (int tt = 0; tt < 2; tt++) xf[tt] = *(LAS v4u*)(sb + ((xoff + tt * 2048) ^ (kk * 64)));
      if (UG == 0) {
#pragma unroll
        for (int u = 0; u < 4; u++) wf[u] = *(LAS v4u*)(sb + ((soff + u * 2048) ^ (kk * 64)));
      }
#pragma unroll
      for (int i = 0; i < NF; i++) {
        const v2u a = trread(sb + woff[i] + kk * 8192), b = trread(sb + woff[i] + kk * 8192 + 1024);
        wf[NU - NF + i] = v4u{a.x, a.y, b.x, b.y};
      }
#pragma unroll
      for (int u = 0; u < NU; u++) {
        acc[u][0] = mfma(wf[u], xf[0], acc[u][0]);
        acc[u][1] = mfma(wf[u], xf[1], acc[u][1]);
      }
      if (UG == 1) {
#pragma unroll
        for (int tt = 0; tt < 2; tt++)
#pragma unroll
          for (int e = 0; e < 4; e++) { sm[tt] = dot2(xf[tt][e], ONES, sm[tt]); sq[tt] = dot2(xf[tt][e], xf[tt][e], sq[tt]); }
      }
      if (((2 * s + kk) & 3) == tg) {
#pragma unroll
        for (int u = 0; u < NU; u++)
#pragma unroll
          for (int e = 0; e < 4; e++) cs[u] = dot2(wf[u][e], ONES, cs[u]);
      }
    }
  }
  ST(4);
  LAS char* SB = L;
  LAS float* CSP = (LAS float*)(L + STAGE);
  LAS f32x2* STT = (LAS f32x2*)(L + STAGE + 4096);
  LAS char* TR = L + STAGE + 8192;
  if (UG == 0) {
#pragma unroll
    for (int u = 0; u < 4; u++)
#pragma unroll
      for (int tt = 0; tt < 2; tt++) {
        const int token = 32 * tg + 16 * tt + tok;
        *(LAS f32x4*)(SB + token * 272 + (token >> 4) * 64 + (8 * u + 2 * g) * 8) = acc[u][tt];
      }
  }
#pragma unroll
  for (int u = 0; u < NU; u++) {
    float v = cs[u]; v += __shfl_xor(v, 16); v += __shfl_xor(v, 32);
    if (lane < 16) CSP[tg * 176 + (UG == 0 ? u : 6 + u) * 16 + lane] = v;
  }
  if (UG == 1) {
#pragma unroll
    for (int tt = 0; tt < 2; tt++) {
      float a = sm[tt], b = sq[tt];
      a += __shfl_xor(a, 16); a += __shfl_xor(a, 32); b += __shfl_xor(b, 16); b += __shfl_xor(b, 32);
      const float m = a * (1.f / 512.f);
      const float var = fmaxf(b * (1.f / 512.f) - m * m, 0.f);
      const float mb = rbf(m), sd = rbf(sqrtf(rbf(rbf(var) + 1e-5f)));
      if (lane < 16) {
        STT[32 * tg + 16 * tt + lane] = f32x2{mb, 1.f / sd};
        if (c == 0) *(f32x2*)(ws + WS_ST + (size_t)(128 * r + 32 * tg + 16 * tt + lane) * 8) = f32x2{mb, sd};
      }
    }
  }
  __syncthreads();
  ST(5);
  cplx bu[16], lam, init0 = {0.f, 0.f}, lseg = {1.f, 0.f}, wa = {0.f, 0.f}, wb = {0.f, 0.f};
  if (UG == 0) {
    const float lr = bf2f(plre), li = bf2f(plim), dt = rbf(expf(bf2f(pldt)));
    const float er = expf(dt * lr); float sn, cn; sincosf(dt * li, &sn, &cn);
    lam = {er * cn, er * sn};
    const float den = 1.f / (lr * lr + li * li), nr = lam.r - 1.f, ni = lam.i;
    const cplx coef = {(nr * lr + ni * li) * den, (ni * lr - nr * li) * den};
    const int nrow = 16 * tg + 2 * sch;
    const float csr = (CSP[nrow] + CSP[176 + nrow]) + (CSP[352 + nrow] + CSP[528 + nrow]);
    const float csi = (CSP[nrow + 1] + CSP[177 + nrow]) + (CSP[353 + nrow] + CSP[529 + nrow]);
    cplx h = {0.f, 0.f};
#pragma unroll
    for (int t = 0; t < 16; t++) {
      const int token = 16 * seg + t;
      const f32x2 a = *(LAS f32x2*)(SB + token * 272 + seg * 64 + (8 * tg + sch) * 8);
      const f32x2 st = STT[token];
      const cplx uu = {(a.x - st.x * csr) * st.y, (a.y - st.x * csi) * st.y};
      bu[t] = cmul(coef, uu);
      h = cfma(lam, h, bu[t]);
    }
    cplx l2 = cmul(lam, lam), l4 = cmul(l2, l2), l8 = cmul(l4, l4), l16 = cmul(l8, l8), l32 = cmul(l16, l16), l64 = cmul(l32, l32),
         l128 = cmul(l64, l64);
    { cplx o = {__shfl_up(h.r, 8), __shfl_up(h.i, 8)}; if (seg >= 1) h = cfma(l16, o, h); }
    { cplx o = {__shfl_up(h.r, 16), __shfl_up(h.i, 16)}; if (seg >= 2) h = cfma(l32, o, h); }
    { cplx o = {__shfl_up(h.r, 32), __shfl_up(h.i, 32)}; if (seg >= 4) h = cfma(l64, o, h); }
    if (seg == 7 && r < 15) {
      const unsigned ur = __builtin_bit_cast(unsigned, h.r), ui = __builtin_bit_cast(unsigned, h.i);
      GAS unsigned long long* gp = (GAS unsigned long long*)(ws + WS_E + ((size_t)r * 512 + chg) * 16);
      __hip_atomic_store(gp, ((unsigned long long)((unsigned)ep + hashA(ur, ui)) << 32) | ur, RLX_AGENT);
      __hip_atomic_store(gp + 1, ((unsigned long long)((unsigned)(ep >> 32) + hashB(ur, ui)) << 32) | ui, RLX_AGENT);
    }
    { cplx o = {__shfl_up(h.r, 8), __shfl_up(h.i, 8)}; if (seg >= 1) init0 = o; }
    if (seg & 1) lseg = l16;
    if (seg & 2) lseg = cmul(lseg, l32);
    if (seg & 4) lseg = cmul(lseg, l64);
    const cplx l256 = cmul(l128, l128), l512 = cmul(l256, l256), l1024 = cmul(l512, l512);
    const int nb = r - 1 - (seg + 8);
    cplx t = {1.f, 0.f};
    const int ne = nb >= 0 ? nb : (nb + 8 >= 0 ? nb + 8 : 0);
    if (ne & 1) t = l128;
    if (ne & 2) t = cmul(t, l256);
    if (ne & 4) t = cmul(t, l512);
    if (nb >= 0) { wb = t; wa = cmul(t, l1024); } else if (nb + 8 >= 0) { wa = t; }
  }
  ST(6);
  {
    char* hb = ws + WS_H;
#pragma unroll
    for (int tt = 0; tt < 2; tt++) {
      const f32x2 st = STT[32 * tg + 16 * tt + tok];
      const int T16 = 8 * r + 2 * tg + tt;
      char* fb = hb + ((size_t)(T16 >> 2) * KS2 * 4 + (T16 & 3)) * 1024 + lane * 16;
      v2u o[NF];
#pragma unroll
      for (int i = 0; i < NF; i++) {
        const int cu = (4 + V0 + i) * 16 + 4 * g;
        const f32x4 c4 = (*(LAS f32x4*)(CSP + cu) + *(LAS f32x4*)(CSP + 176 + cu)) + (*(LAS f32x4*)(CSP + 352 + cu) + *(LAS f32x4*)(CSP + 528 + cu));
        float hv[4];
#pragma unroll
        for (int e = 0; e < 4; e++) hv[e] = silu_f(rbf((acc[NU - NF + i][tt][e] - st.x * c4[e]) * st.y));
        o[i] = v2u{pkbf(hv[0], hv[1]), pkbf(hv[2], hv[3])};
      }
#pragma unroll
      for (int i = 0; i + 1 < NF; i += 2) {
        const int U = 7 * c + V0 + i + codd;
        *(v4u*)(fb + (size_t)(U >> 1) * 4096) = v4u{o[i].x, o[i].y, o[i + 1].x, o[i + 1].y};
      }
      if (UG == 1) {
        const int U = codd ? 7 * c : 7 * c + 6;
        *(v2u*)(fb + (size_t)(U >> 1) * 4096 + (U & 1) * 8) = o[NF - 1];
      }
    }
  }
  ST(7);
  if (UG == 0) {
    cplx carry = {0.f, 0.f};
    if (r > 0) {
      const bool needA = seg < r, needB = seg + 8 < r;
      GAS unsigned long long* ga = (GAS unsigned long long*)(ws + WS_E + ((size_t)seg * 512 + chg) * 16);
      GAS unsigned long long* gb = ga + (size_t)8 * 512 * 2;
      unsigned long long a0 = 0, a1 = 0, b0 = 0, b1 = 0;
      for (unsigned spins = 0; spins < SPIN_CAP; ++spins) {
        bool ok = true;
        if (needA) {
          a0 = __hip_atomic_load(ga, RLX_AGENT); a1 = __hip_atomic_load(ga + 1, RLX_AGENT);
          ok = (unsigned)(a0 >> 32) == (unsigned)ep + hashA((unsigned)a0, (unsigned)a1) &&
               (unsigned)(a1 >> 32) == (unsigned)(ep >> 32) + hashB((unsigned)a0, (unsigned)a1);
        }
        if (needB) {
          b0 = __hip_atomic_load(gb, RLX_AGENT); b1 = __hip_atomic_load(gb + 1, RLX_AGENT);
          ok = ok && (unsigned)(b0 >> 32) == (unsigned)ep + hashA((unsigned)b0, (unsigned)b1) &&
               (unsigned)(b1 >> 32) == (unsigned)(ep >> 32) + hashB((unsigned)b0, (unsigned)b1);
        }
        if (__all(ok)) break;
        __builtin_amdgcn_s_sleep(1);
      }
      cplx ea = {__builtin_bit_cast(float, (unsigned)a0), __builtin_bit_cast(float, (unsigned)a1)};
      cplx eb = {__builtin_bit_cast(float, (unsigned)b0), __builtin_bit_cast(float, (unsigned)b1)};
      cplx s0 = {0.f, 0.f};
      if (needA) s0 = cmul(wa, ea);
      if (needB) s0 = cfma(wb, eb, s0);
      s0.r += __shfl_xor(s0.r, 8); s0.i += __shfl_xor(s0.i, 8);
      s0.r += __shfl_xor(s0.r, 16); s0.i += __shfl_xor(s0.i, 16);
      s0.r += __shfl_xor(s0.r, 32); s0.i += __shfl_xor(s0.i, 32);
      carry = s0;
      if (r == 15 && lane == 0) __hip_atomic_store((GAS unsigned long long*)(ws + WS_EP + (size_t)(c * 4 + tg) * 128), ep, RLX_AGENT);
    }
    ST(8);
    cplx h = cfma(lseg, carry, init0);
    LAS char* tr = TR + tg * 4224;
#pragma unroll
    for (int t = 0; t < 16; t++) {
      h = cfma(lam, h, bu[t]);
      const unsigned pk = pkbf(h.r, -h.i);
      LAS u16* dst = (LAS u16*)(tr + seg * 528 + t * 32 + sch * 2);
      dst[0] = (u16)pk; dst[8] = (u16)(pk >> 16);
    }
    asm volatile("" ::: "memory");
    char* hb = ws + WS_H;
#pragma unroll
    for (int j = 0; j < 4; j++) {
      const int part = j & 1, token = (j >> 1) * 64 + lane;
      const v4u v = *(LAS v4u*)(tr + (token >> 4) * 528 + (token & 15) * 32 + part * 16);
      const int T16 = 8 * r + (token >> 4);
      *(v4u*)(hb + (((size_t)(T16 >> 2) * KS2 + 56 + 2 * c + part) * 4 + (T16 & 3)) * 1024 + ((token & 15) + 16 * tg) * 16) = v;
    }
  }
  ST(9);
  ST_FLUSH(0, ws);
}

__global__ __launch_bounds__(512, 2) void k_p1(const u16* __restrict__ x, const u16* __restrict__ Lre, const u16* __restrict__ Lim,
                                               const u16* __restrict__ Bre, const u16* __restrict__ Bim, const u16* __restrict__ logdt,
                                               const u16* __restrict__ w1, char* __restrict__ ws) {
  extern __shared__ __attribute__((aligned(16))) char smem[];
  LAS char* L = (LAS char*)smem;
  const int b = blockIdx.x, xcd = b & 7, idx = b >> 3;
  const int r = (xcd & 1) * 8 + (idx & 7), c = (xcd >> 1) * 4 + (idx >> 3);
  const int w = __builtin_amdgcn_readfirstlane(threadIdx.x >> 6);
  if (w < 4) p1_body<0>(x, Lre, Lim, Bre, Bim, logdt, w1, ws, L, r, c, w);
  else p1_body<1>(x, Lre, Lim, Bre, Bim, logdt, w1, ws, L, r, c, w);
}

constexpr int W2 = 4;
__device__ __forceinline__ void aload4(v4u (&r)[4], unsigned voff, const char* sbase) {
  asm volatile("global_load_dwordx4 %0, %4, %5\n\tglobal_load_dwordx4 %1, %4, %5 offset:1024\n\t"
               "global_load_dwordx4 %2, %4, %5 offset:2048\n\tglobal_load_dwordx4 %3, %4, %5 offset:3072"
               : "=&v"(r[0]), "=&v"(r[1]), "=&v"(r[2]), "=&v"(r[3]) : "v"(voff), "s"(sbase) : "memory");
}
__device__ __forceinline__ void trread8(v4u (&wf)[4], unsigned a0, unsigned a1, unsigned a2, unsigned a3) {
  v2u x0, x1, x2, x3, x4, x5, x6, x7;
  asm volatile("ds_read_b64_tr_b16 %0, %8\n\tds_read_b64_tr_b16 %1, %8 offset:2048\n\t"
               "ds_read_b64_tr_b16 %2, %9\n\tds_read_b64_tr_b16 %3, %9 offset:2048\n\t"
               "ds_read_b64_tr_b16 %4, %10\n\tds_read_b64_tr_b16 %5, %10 offset:2048\n\t"
               "ds_read_b64_tr_b16 %6, %11\n\tds_read_b64_tr_b16 %7, %11 offset:2048\n\ts_waitcnt lgkmcnt(0)"
               : "=&v"(x0), "=&v"(x1), "=&v"(x2), "=&v"(x3), "=&v"(x4), "=&v"(x5), "=&v"(x6), "=&v"(x7)
               : "v"(a0), "v"(a1), "v"(a2), "v"(a3) : "memory");
  wf[0] = v4u{x0.x, x0.y, x1.x, x1.y}; wf[1] = v4u{x2.x, x2.y, x3.x, x3.y};
  wf[2] = v4u{x4.x, x4.y, x5.x, x5.y}; wf[3] = v4u{x6.x, x6.y, x7.x, x7.y};
}
__global__ __launch_bounds__(512, 2) void k_p2(const u16* __restrict__ x, const u16* __restrict__ Cre, const u16* __restrict__ Cim,
                                               const u16* __restrict__ Dsk, const u16* __restrict__ w2, const char* __restrict__ ws,
                                               float* __restrict__ out) {
  extern __shared__ __attribute__((aligned(16))) char smem[];
  LAS char* L = (LAS char*)smem;
  const int b = blockIdx.x, xcd = b & 7, idx = b >> 3;
  const int rt = 4 * xcd + (idx & 3), ct = idx >> 2;
  const int tid = threadIdx.x, lane = tid & 63, w = __builtin_amdgcn_readfirstlane(tid >> 6);
  const int tok = lane & 15, g = lane >> 4;
  v2u ex[2], ed[2]; f32x2 est[2];
#pragma unroll
  for (int j = 0; j < 2; j++) {
    const int it = tid + 512 * j, token = 64 * rt + (it >> 4), d = 64 * ct + 4 * (it & 15);
    ex[j] = *(const v2u*)(x + (size_t)token * D_ + d);
    ed[j] = *(const v2u*)(Dsk + d);
    est[j] = *(const f32x2*)(ws + WS_ST + (size_t)token * 8);
  }
  LAS char* ring = L + w * 16384;
  const int s0 = 11 * w;
  const char* ap = ws + WS_H + ((size_t)rt * KS2 + s0) * 4096;
  ST_DECL ST(0);
  const int r8 = lane >> 3, p8 = lane & 7;
  const u16* bw = w2 + (size_t)r8 * D_ + 64 * ct + (((((p8 >> 1) ^ ((r8 >> 1) & 3)) << 1) | (p8 & 1)) << 3);
  const int r4 = lane >> 2, c4 = lane & 3;
  const size_t bc = (size_t)(64 * ct + r4) * 512 + ((c4 ^ ((-(r4 >> 2)) & 3)) << 3);
  auto issueB = [&](int i) {
    const int ks = s0 + i;
    LAS char* sl = ring + (i & 3) * 4096;
    if (ks < 56) {
#pragma unroll
      for (int j = 0; j < 4; j++) glds16(bw + (size_t)(32 * ks + 8 * j) * D_, sl + j * 1024);
    } else {
      const u16* cb = (((ks - 56) & 1) ? Cim : Cre) + bc + 32 * ((ks - 56) >> 1);
#pragma unroll
      for (int j = 0; j < 4; j++) glds16(cb + (size_t)j * 16 * 512, sl + j * 1024);
    }
  };
  v4u ra[W2][4];
  auto issueA = [&](int i) {
    aload4(ra[i & 3], lane * 16, ap + (size_t)i * 4096);
  };
#pragma unroll
  for (int i = 0; i < W2; i++) { issueB(i); issueA(i); }
  const int q = tok >> 2, p4 = lane & 3;
  const int fw = (2 * g + (q >> 1)) & 3;
  int toff[4];
#pragma unroll
  for (int j = 0; j < 4; j++) toff[j] = (4 * g + q) * 128 + ((j ^ fw) << 5) + p4 * 8;
  const int coff = tok * 64 + ((g ^ ((-(tok >> 2)) & 3)) << 4);
  f32x4 accX[4][4], accY[4][4];
#pragma unroll
  for (int i = 0; i < 4; i++)
#pragma unroll
    for (int j = 0; j < 4; j++) { accX[i][j] = f32x4{0, 0, 0, 0}; accY[i][j] = f32x4{0, 0, 0, 0}; }
#pragma unroll
  for (int i = 0; i < 11; i++) {
    if (i <= 7) WAITV(24); else if (i == 8) WAITV(16); else if (i == 9) WAITV(8); else WAITV(0);
    asm volatile("" : "+v"(ra[i & 3][0]), "+v"(ra[i & 3][1]), "+v"(ra[i & 3][2]), "+v"(ra[i & 3][3]));
    if (i < 11) ST(1 + i);
    LAS char* sl = ring + (i & 3) * 4096;
    v4u wf[4];
    if (s0 + i < 56) {
      const unsigned sb = (unsigned)(size_t)sl;
      trread8(wf, sb + toff[0], sb + toff[1], sb + toff[2], sb + toff[3]);
    } else {
#pragma unroll
      for (int j = 0; j < 4; j++) wf[j] = *(LAS v4u*)(sl + coff + j * 1024);
    }
#pragma unroll
    for (int j = 0; j < 4; j++)
#pragma unroll
      for (int t = 0; t < 4; t++) {
        if (i == 0) accX[j][t] = mfma(wf[j], ra[i & 3][t], accX[j][t]);
        else accY[j][t] = mfma(wf[j], ra[i & 3][t], accY[j][t]);
      }
    __builtin_amdgcn_sched_barrier(0);
    if (i + W2 < 11) { issueB(i + W2); issueA(i + W2); }
    __builtin_amdgcn_sched_barrier(0);
  }
  ST(12);
  {
    LAS char* px = (w == 5) ? (L + 8 * 16384) : ring;
#pragma unroll
    for (int j = 0; j < 4; j++)
#pragma unroll
      for (int t = 0; t < 4; t++) {
        const int o = ((t * 4 + j) * 4 + g) * 256 + ((tok + 4 * j + g) & 15) * 16;
        if (w == 5) { *(LAS f32x4*)(px + o) = accX[j][t]; *(LAS f32x4*)(ring + o) = accY[j][t]; }
        else *(LAS f32x4*)(ring + o) = accX[j][t] + accY[j][t];
      }
  }
  __syncthreads();
  ST(13);
#pragma unroll
  for (int j = 0; j < 2; j++) {
    const int it = tid + 512 * j, tl = it >> 4, dq = it & 15;
    const int o = (((tl >> 4) * 4 + (dq >> 2)) * 4 + (dq & 3)) * 256 + (((tl & 15) + dq) & 15) * 16;
    f32x4 ff = *(LAS f32x4*)(L + o);
#pragma unroll
    for (int p = 1; p < 5; p++) ff += *(LAS f32x4*)(L + p * 16384 + o);
    ff += *(LAS f32x4*)(L + 8 * 16384 + o);
    f32x4 sy = *(LAS f32x4*)(L + 5 * 16384 + o) + *(LAS f32x4*)(L + 6 * 16384 + o) + *(LAS f32x4*)(L + 7 * 16384 + o);
    f32x4 res;
#pragma unroll
    for (int e = 0; e < 4; e++) {
      const unsigned xw = e < 2 ? ex[j].x : ex[j].y, dw = e < 2 ? ed[j].x : ed[j].y;
      const float xv = bf2f((e & 1) ? xw >> 16 : xw & 0xffffu), dv = bf2f((e & 1) ? dw >> 16 : dw & 0xffffu);
      const float xn = rbf(rbf(xv - est[j].x) / est[j].y);
      const float s = 2.f * sy[e] + rbf(xn * dv);
      const float gg = rbf(silu_f(rbf(ff[e])));
      res[e] = xv + s * gg;
    }
    *(f32x4*)(out + (size_t)(64 * rt + tl) * D_ + 64 * ct + 4 * dq) = res;
  }
  ST(14);
  ST_FLUSH(1, ws);
}
}

extern "C" void kernel_launch(void* const* d_in, const int* in_sizes, int n_in, void* d_out, int out_size, void* d_ws, size_t ws_size,
                              hipStream_t stream) {
  const u16* x = (const u16*)d_in[0]; const u16* Lre = (const u16*)d_in[1]; const u16* Lim = (const u16*)d_in[2];
  const u16* Bre = (const u16*)d_in[3]; const u16* Bim = (const u16*)d_in[4]; const u16* Cre = (const u16*)d_in[5];
  const u16* Cim = (const u16*)d_in[6]; const u16* logdt = (const u16*)d_in[7]; const u16* Dsk = (const u16*)d_in[8];
  const u16* w1 = (const u16*)d_in[10]; const u16* w2 = (const u16*)d_in[11];
  (void)hipFuncSetAttribute((const void*)k_p1, hipFuncAttributeMaxDynamicSharedMemorySize, LDS_BYTES);
  (void)hipFuncSetAttribute((const void*)k_p2, hipFuncAttributeMaxDynamicSharedMemorySize, LDS_BYTES);
  k_p1<<<256, 512, LDS_BYTES, stream>>>(x, Lre, Lim, Bre, Bim, logdt, w1, (char*)d_ws);
  k_p2<<<256, 512, 9 * 16384, stream>>>(x, Cre, Cim, Dsk, w2, (const char*)d_ws, (float*)d_out);
}
```

```cpp
#include <hip/hip_runtime.h>
#include <hip/hip_bf16.h>
#include <stdint.h>
#include <utility>

typedef unsigned short u16;
typedef unsigned long long u64;
typedef unsigned v4u __attribute__((ext_vector_type(4)));
typedef unsigned v2u __attribute__((ext_vector_type(2)));
typedef float f32x4 __attribute__((ext_vector_type(4)));
typedef float f32x2 __attribute__((ext_vector_type(2)));
typedef short bf16x8 __attribute__((ext_vector_type(8)));
typedef short v4s __attribute__((ext_vector_type(4)));
typedef __bf16 v2bf __attribute__((ext_vector_type(2)));
#define LAS __attribute__((address_space(3)))
#define GAS __attribute__((address_space(1)))
#define RLX_AGENT __ATOMIC_RELAXED, __HIP_MEMORY_SCOPE_AGENT
#define RLX_WG __ATOMIC_RELAXED, __HIP_MEMORY_SCOPE_WORKGROUP

namespace {
constexpr int D_ = 512, F_ = 1792;
constexpr int KS2 = 88;
constexpr size_t WS_H = 0;
constexpr size_t WS_H_BYTES = (size_t)32 * KS2 * 4096;
constexpr size_t WS_BT = WS_H + WS_H_BYTES;
constexpr size_t WS_ST = WS_BT + (size_t)8 * KS2 * 4096;
constexpr size_t WS_CP = WS_ST + 2048 * 4;
constexpr size_t WS_E = WS_CP + 16 * 16 * 448;
constexpr size_t WS_FL = WS_E + 16 * 512 * 16;
constexpr size_t WS_EF = WS_FL + 16 * 256;
constexpr size_t WS_EG = WS_EF + 256 * 8;
constexpr size_t WS_EP = WS_EG + 256 * 8;
constexpr size_t WS_END = WS_EP + 128;
constexpr int SLOT = 32768, OFF_W = 16384;
constexpr int M_STT = 131072, M_CS = M_STT + 1024, M_PRM = M_CS + 512, M_CNT = M_PRM + 512, M_TR = M_CNT + 256;
constexpr int LDS_BYTES = 163840;
constexpr unsigned SPIN_CAP = 1u << 17;
#define FAUX 16
constexpr int AUX_SC1 = 16;
#define PSLEEP 6

__device__ __forceinline__ float bf2f(unsigned b) { return __builtin_bit_cast(float, b << 16); }
__device__ __forceinline__ float bfhi(unsigned b) { return __builtin_bit_cast(float, b & 0xffff0000u); }
__device__ __forceinline__ unsigned pkbf(float lo, float hi) {
  v2bf v; v.x = (__bf16)lo; v.y = (__bf16)hi; return __builtin_bit_cast(unsigned, v);
}
__device__ __forceinline__ float rbf(float f) { return bf2f(pkbf(f, 0.f)); }
__device__ __forceinline__ f32x4 mfma(v4u a, v4u b, f32x4 c) {
  return __builtin_amdgcn_mfma_f32_16x16x32_bf16(__builtin_bit_cast(bf16x8, a), __builtin_bit_cast(bf16x8, b), c, 0, 0, 0);
}
__device__ __forceinline__ float dot2(unsigned a, unsigned b, float c) {
  return __builtin_amdgcn_fdot2_f32_bf16(__builtin_bit_cast(v2bf, a), __builtin_bit_cast(v2bf, b), c, false);
}
__device__ __forceinline__ void glds16(const void* g, LAS char* l) {
  __builtin_amdgcn_global_load_lds((const GAS void*)g, (LAS void*)l, 16, 0, 0);
}
__device__ __forceinline__ void glds16c(const void* g, LAS char* l) {
  __builtin_amdgcn_global_load_lds((const GAS void*)g, (LAS void*)l, 16, 0, 16);
}
__device__ __forceinline__ v2u trread(LAS char* p) {
  return __builtin_bit_cast(v2u, __builtin_amdgcn_ds_read_tr16_b64_v4i16((LAS v4s*)p));
}
__device__ __forceinline__ float silu_f(float h) { return h * __builtin_amdgcn_rcpf(1.f + __builtin_amdgcn_exp2f(h * -1.44269504088896f)); }
__device__ __forceinline__ unsigned silu2(unsigned hb) {
  const f32x2 h = {bf2f(hb), bfhi(hb)};
  const f32x2 t = h * -1.44269504088896f;
  const f32x2 d = f32x2{__builtin_amdgcn_exp2f(t.x), __builtin_amdgcn_exp2f(t.y)} + 1.f;
  const f32x2 y = h * f32x2{__builtin_amdgcn_rcpf(d.x), __builtin_amdgcn_rcpf(d.y)};
  return pkbf(y.x, y.y);
}
struct cplx { float r, i; };
__device__ __forceinline__ cplx cmul(cplx a, cplx b) { return {__builtin_fmaf(a.r, b.r, -a.i * b.i), __builtin_fmaf(a.r, b.i, a.i * b.r)}; }
__device__ __forceinline__ cplx cfma(cplx a, cplx b, cplx c) {
  return {__builtin_fmaf(a.r, b.r, __builtin_fmaf(-a.i, b.i, c.r)), __builtin_fmaf(a.r, b.i, __builtin_fmaf(a.i, b.r, c.i))};
}
__device__ __forceinline__ unsigned hashA(unsigned a, unsigned b) { unsigned h = a * 0x9E3779B1u + (b ^ (b >> 15)) * 0x85EBCA77u; return h ^ (h >> 13); }
__device__ __forceinline__ unsigned hashB(unsigned a, unsigned b) { unsigned h = b * 0xC2B2AE3Du + (a ^ (a >> 16)) * 0x27D4EB2Fu; return h ^ (h >> 15); }
template <int N> __device__ __forceinline__ void waitv() { asm volatile("s_waitcnt vmcnt(%0)" ::"n"(N) : "memory"); }
#define WAITL0() __builtin_amdgcn_s_waitcnt(0xC07F)
#define BAR() __builtin_amdgcn_s_barrier()
#define SB0() __builtin_amdgcn_sched_barrier(0)
#define CBAR() asm volatile("" ::: "memory")
#define ST_DECL
#define ST(i)
#define SF(i)
#define ST_FLUSH(wsp)

__device__ __forceinline__ unsigned lds_arrive(LAS unsigned* c) {
  unsigned o = 0;
  if ((threadIdx.x & 63) == 0)
    asm volatile("ds_add_rtn_u32 %0, %1, %2\n\ts_waitcnt lgkmcnt(0)" : "=&v"(o) : "v"((unsigned)(size_t)c), "v"(1u) : "memory");
  return __builtin_amdgcn_readfirstlane(o);
}
__device__ __forceinline__ void lds_wait(LAS unsigned* c, unsigned want) {
  for (unsigned spins = 0; spins < (1u << 22); ++spins) {
    unsigned v;
    asm volatile("ds_read_b32 %0, %1\n\ts_waitcnt lgkmcnt(0)" : "=&v"(v) : "v"((unsigned)(size_t)c) : "memory");
    if (__builtin_amdgcn_readfirstlane(v) >= want) break;
    __builtin_amdgcn_s_sleep(1);
  }
}
struct Tr14 { v2u h[14]; };
template <int OFF> __device__ __forceinline__ void tr14(Tr14& t, const unsigned (&ad)[7]) {
  asm volatile("ds_read_b64_tr_b16 %0, %14 offset:%21\n\tds_read_b64_tr_b16 %1, %14 offset:%22\n\t"
               "ds_read_b64_tr_b16 %2, %15 offset:%21\n\tds_read_b64_tr_b16 %3, %15 offset:%22\n\t"
               "ds_read_b64_tr_b16 %4, %16 offset:%21\n\tds_read_b64_tr_b16 %5, %16 offset:%22\n\t"
               "ds_read_b64_tr_b16 %6, %17 offset:%21\n\tds_read_b64_tr_b16 %7, %17 offset:%22\n\t"
               "ds_read_b64_tr_b16 %8, %18 offset:%21\n\tds_read_b64_tr_b16 %9, %18 offset:%22\n\t"
               "ds_read_b64_tr_b16 %10, %19 offset:%21\n\tds_read_b64_tr_b16 %11, %19 offset:%22\n\t"
               "ds_read_b64_tr_b16 %12, %20 offset:%21\n\tds_read_b64_tr_b16 %13, %20 offset:%22"
               : "=&v"(t.h[0]), "=&v"(t.h[1]), "=&v"(t.h[2]), "=&v"(t.h[3]), "=&v"(t.h[4]), "=&v"(t.h[5]), "=&v"(t.h[6]),
                 "=&v"(t.h[7]), "=&v"(t.h[8]), "=&v"(t.h[9]), "=&v"(t.h[10]), "=&v"(t.h[11]), "=&v"(t.h[12]), "=&v"(t.h[13])
               : "v"(ad[0]), "v"(ad[1]), "v"(ad[2]), "v"(ad[3]), "v"(ad[4]), "v"(ad[5]), "v"(ad[6]), "n"(OFF), "n"(OFF + 1024)
               : "memory");
}
template <int OFF> __device__ __forceinline__ void rd2x(v4u (&x)[2], unsigned ad) {
  asm volatile("ds_read_b128 %0, %2 offset:%3\n\tds_read_b128 %1, %2 offset:%4" : "=&v"(x[0]), "=&v"(x[1]) : "v"(ad), "n"(OFF), "n"(OFF + 2048) : "memory");
}
template <int N> __device__ __forceinline__ void waitl_pin(Tr14& t, v4u (&x)[2]) {
  asm volatile("s_waitcnt lgkmcnt(%16)"
               : "+v"(t.h[0]), "+v"(t.h[1]), "+v"(t.h[2]), "+v"(t.h[3]), "+v"(t.h[4]), "+v"(t.h[5]), "+v"(t.h[6]), "+v"(t.h[7]), "+v"(t.h[8]),
                 "+v"(t.h[9]), "+v"(t.h[10]), "+v"(t.h[11]), "+v"(t.h[12]), "+v"(t.h[13]), "+v"(x[0]), "+v"(x[1])
               : "n"(N) : "memory");
}
template <int OFF> __device__ __forceinline__ void tr2(v2u& h0, v2u& h1, unsigned ad) {
  asm volatile("ds_read_b64_tr_b16 %0, %2 offset:%3\n\tds_read_b64_tr_b16 %1, %2 offset:%4" : "=&v"(h0), "=&v"(h1) : "v"(ad), "n"(OFF), "n"(OFF + 1024) : "memory");
}
__device__ __forceinline__ void keep(const Tr14& t, const v4u (&x)[2]) {
  asm volatile("" :: "v"(t.h[0]), "v"(t.h[1]), "v"(t.h[2]), "v"(t.h[3]), "v"(t.h[4]), "v"(t.h[5]), "v"(t.h[6]), "v"(t.h[7]), "v"(t.h[8]),
               "v"(t.h[9]), "v"(t.h[10]), "v"(t.h[11]), "v"(t.h[12]), "v"(t.h[13]), "v"(x[0]), "v"(x[1]));
}
__device__ __forceinline__ void waitl_pinS(v4u (&x)[2], v2u (&t)[4]) {
  asm volatile("s_waitcnt lgkmcnt(0)" : "+v"(x[0]), "+v"(x[1]), "+v"(t[0]), "+v"(t[1]), "+v"(t[2]), "+v"(t[3]) :: "memory");
}
template <int OFF> __device__ __forceinline__ void tr4(v2u (&h)[4], unsigned a0, unsigned a1) {
  asm volatile("ds_read_b64_tr_b16 %0, %4 offset:%6\n\tds_read_b64_tr_b16 %1, %4 offset:%7\n\t"
               "ds_read_b64_tr_b16 %2, %5 offset:%6\n\tds_read_b64_tr_b16 %3, %5 offset:%7"
               : "=&v"(h[0]), "=&v"(h[1]), "=&v"(h[2]), "=&v"(h[3]) : "v"(a0), "v"(a1), "n"(OFF), "n"(OFF + 1024) : "memory");
}
template <int OFF> __device__ __forceinline__ void rd4w(v4u (&x)[4], unsigned ad) {
  asm volatile("ds_read_b128 %0, %4 offset:%5\n\tds_read_b128 %1, %4 offset:%6\n\tds_read_b128 %2, %4 offset:%7\n\tds_read_b128 %3, %4 offset:%8"
               : "=&v"(x[0]), "=&v"(x[1]), "=&v"(x[2]), "=&v"(x[3]) : "v"(ad), "n"(OFF), "n"(OFF + 2048), "n"(OFF + 4096), "n"(OFF + 6144) : "memory");
}
template <int OFF> __device__ __forceinline__ void rd1(v4u& x, unsigned ad) {
  asm volatile("ds_read_b128 %0, %1 offset:%2" : "=&v"(x) : "v"(ad), "n"(OFF) : "memory");
}
template <int N> __device__ __forceinline__ void waitl_pin5(v4u (&x)[4], v4u& y) {
  asm volatile("s_waitcnt lgkmcnt(%5)" : "+v"(x[0]), "+v"(x[1]), "+v"(x[2]), "+v"(x[3]), "+v"(y) : "n"(N) : "memory");
}
template <int N> __device__ __forceinline__ void waitl_pin4(v4u (&x)[4]) {
  asm volatile("s_waitcnt lgkmcnt(%4)" : "+v"(x[0]), "+v"(x[1]), "+v"(x[2]), "+v"(x[3]) : "n"(N) : "memory");
}
__device__ __forceinline__ float fdiv(float a, float b) {
  const float rc = __builtin_amdgcn_rcpf(b), q0 = a * rc;
  return __builtin_fmaf(__builtin_fmaf(-q0, b, a), rc, q0);
}
typedef std::integral_constant<int, 0> I0;
#define IC(n) std::integral_constant<int, (n)>{}
#define CAS __attribute__((address_space(4)))
__device__ __forceinline__ void waitl_pin2(v4u (&x)[2]) { asm volatile("s_waitcnt lgkmcnt(0)" : "+v"(x[0]), "+v"(x[1]) :: "memory"); }
__device__ __forceinline__ void pin4(v4u (&x)[4]) { asm volatile("" : "+v"(x[0]), "+v"(x[1]), "+v"(x[2]), "+v"(x[3])); }
__device__ __forceinline__ void rd4k(v4u (&x)[4], unsigned ad) {
  asm volatile("ds_read_b128 %0, %4\n\tds_read_b128 %1, %4 offset:1024\n\tds_read_b128 %2, %4 offset:2048\n\tds_read_b128 %3, %4 offset:3072\n\ts_waitcnt lgkmcnt(0)"
               : "=&v"(x[0]), "=&v"(x[1]), "=&v"(x[2]), "=&v"(x[3]) : "v"(ad) : "memory");
}
__device__ __forceinline__ void xload8(v4u (&x)[2][4], const char* p) {
  asm volatile("global_load_dwordx4 %0, %8, off\n\tglobal_load_dwordx4 %1, %8, off offset:128\n\tglobal_load_dwordx4 %2, %8, off offset:256\n\t"
               "global_load_dwordx4 %3, %8, off offset:384\n\tglobal_load_dwordx4 %4, %8, off offset:512\n\tglobal_load_dwordx4 %5, %8, off offset:640\n\t"
               "global_load_dwordx4 %6, %8, off offset:768\n\tglobal_load_dwordx4 %7, %8, off offset:896"
               : "=&v"(x[0][0]), "=&v"(x[0][1]), "=&v"(x[0][2]), "=&v"(x[0][3]), "=&v"(x[1][0]), "=&v"(x[1][1]), "=&v"(x[1][2]), "=&v"(x[1][3]) : "v"(p) : "memory");
}
__device__ __forceinline__ void pload3(unsigned (&v)[3], unsigned off, const void* p0, const void* p1, const void* p2) {
  asm volatile("global_load_ushort %0, %3, %4\n\tglobal_load_ushort %1, %3, %5\n\tglobal_load_ushort %2, %3, %6"
               : "=&v"(v[0]), "=&v"(v[1]), "=&v"(v[2]) : "v"(off), "s"(p0), "s"(p1), "s"(p2) : "memory");
}
__device__ __forceinline__ void efload(v2u (&f)[4], const char* p) {
  asm volatile("global_load_dwordx2 %0, %4, off sc1\n\tglobal_load_dwordx2 %1, %4, off offset:512 sc1\n\t"
               "global_load_dwordx2 %2, %4, off offset:1024 sc1\n\tglobal_load_dwordx2 %3, %4, off offset:1536 sc1"
               : "=&v"(f[0]), "=&v"(f[1]), "=&v"(f[2]), "=&v"(f[3]) : "v"(p) : "memory");
}
__device__ __forceinline__ void efwait(v2u (&f)[4]) { asm volatile("s_waitcnt vmcnt(0)" : "+v"(f[0]), "+v"(f[1]), "+v"(f[2]), "+v"(f[3]) :: "memory"); }
__device__ __forceinline__ void poll_flags(const char* p, int n, u64 tag) {
  const int lane = threadIdx.x & 63;
  GAS u64* f = (GAS u64*)p + (lane < n ? lane : 0);
  for (unsigned spins = 0; spins < SPIN_CAP; ++spins) {
    if (__all(__hip_atomic_load(f, RLX_AGENT) == tag)) break;
    __builtin_amdgcn_s_sleep(PSLEEP);
  }
  __builtin_amdgcn_fence(__ATOMIC_ACQUIRE, "wavefront");
}
__device__ __forceinline__ void aload4(v4u (&r)[4], unsigned voff, const char* sbase) {
  asm volatile("global_load_dwordx4 %0, %4, %5 sc1\n\tglobal_load_dwordx4 %1, %4, %5 offset:1024 sc1\n\t"
               "global_load_dwordx4 %2, %4, %5 offset:2048 sc1\n\tglobal_load_dwordx4 %3, %4, %5 offset:3072 sc1"
               : "=&v"(r[0]), "=&v"(r[1]), "=&v"(r[2]), "=&v"(r[3]) : "v"(voff), "s"(sbase) : "memory");
}
__device__ __forceinline__ void trread8(v4u (&wf)[4], unsigned a0, unsigned a1, unsigned a2, unsigned a3) {
  v2u x0, x1, x2, x3, x4, x5, x6, x7;
  asm volatile("ds_read_b64_tr_b16 %0, %8\n\tds_read_b64_tr_b16 %1, %8 offset:2048\n\t"
               "ds_read_b64_tr_b16 %2, %9\n\tds_read_b64_tr_b16 %3, %9 offset:2048\n\t"
               "ds_read_b64_tr_b16 %4, %10\n\tds_read_b64_tr_b16 %5, %10 offset:2048\n\t"
               "ds_read_b64_tr_b16 %6, %11\n\tds_read_b64_tr_b16 %7, %11 offset:2048\n\ts_waitcnt lgkmcnt(0)"
               : "=&v"(x0), "=&v"(x1), "=&v"(x2), "=&v"(x3), "=&v"(x4), "=&v"(x5), "=&v"(x6), "=&v"(x7)
               : "v"(a0), "v"(a1), "v"(a2), "v"(a3) : "memory");
  wf[0] = v4u{x0.x, x0.y, x1.x, x1.y}; wf[1] = v4u{x2.x, x2.y, x3.x, x3.y};
  wf[2] = v4u{x4.x, x4.y, x5.x, x5.y}; wf[3] = v4u{x6.x, x6.y, x7.x, x7.y};
}
constexpr int p2_refill(int s, bool ffn) { return ffn ? (s < 3 ? 7 + s : (s >= 7 && s <= 10) ? s + 3 : -1) : (s == 0 ? 7 : -1); }
constexpr int p2_wait(int i, int N, int W, bool ffn) {
  int total = 0, need = 0;
  for (int j = 0; j < W && j < N; j++) { total += 4; if (j == i) need = total; }
  for (int s = 0; s < i; s++) {
    if (p2_refill(s, ffn) >= 0) { total += 4; if (p2_refill(s, ffn) == i) need = total; }
    if (s + W < N) { total += 4; if (s + W == i) need = total; }
  }
  return total - need;
}

template <int... I, class Fn> __device__ __forceinline__ void static_for_(std::integer_sequence<int, I...>, Fn&& f) { (f(std::integral_constant<int, I>{}), ...); }
template <int N, class Fn> __device__ __forceinline__ void static_for(Fn&& f) { static_for_(std::make_integer_sequence<int, N>{}, f); }

struct Args {
  const u16 *x, *Lre, *Lim, *Bre, *Bim, *Cre, *Cim, *logdt, *Dsk, *w1, *w2;
  char* ws; float* out;
};

template <bool FFN>
__device__ __forceinline__ void body(const Args& a, LAS char* L, const int w) {
  const int b = blockIdx.x, xcd = b & 7, idx = b >> 3;
  const int r = (xcd & 1) * 8 + (idx & 7), c = (xcd >> 1) * 4 + (idx >> 3);
  const int rt = 4 * xcd + (idx & 3), ct = idx >> 2;
  const int tid = threadIdx.x, lane = tid & 63, tok = lane & 15, g = lane >> 4, tg = w & 3;
  const int codd = c & 1;
  char* const ws = a.ws;
  LAS unsigned* CNT = (LAS unsigned*)(L + M_CNT);
  ST_DECL ST(0);
  const __amdgpu_buffer_rsrc_t rsw = __builtin_amdgcn_make_buffer_rsrc((void*)ws, 0, (int)WS_END, 0x00020000);
  const int r8 = lane >> 3, p8 = lane & 7;
  constexpr int N2 = FFN ? 14 : 8, WA = FFN ? 5 : 4, NL = FFN ? 3 : 1, NRB = FFN ? 4 : 6;
  const int s0 = FFN ? 14 * tg : 56 + 8 * tg;
  const char* bt = ws + WS_BT + (size_t)ct * KS2 * 4096;
  auto ringp = [&](int i) { return L + (FFN ? 65536 + tg * 12288 + (i < 3 ? i : (i - 7) % 3) * 4096 : 114688 + tg * 4096); };
  const int swz = (tok >> 1) & 7;
  const int xoff = (32 * tg + tok) * 128 + ((g ^ swz) << 4);
  const int q = tok >> 2, p4 = lane & 3, fl = q | ((g & 1) << 2);
  const unsigned lb = (unsigned)(size_t)L;
  const unsigned xad[4] = {lb + xoff, lb + (xoff ^ 64), lb + xoff + 2 * SLOT, lb + (xoff ^ 64) + 2 * SLOT};
  constexpr unsigned ONES = 0x3f803f80u;
  const v4u ones = {ONES, ONES, ONES, ONES};
  LAS f32x2* STT = (LAS f32x2*)(L + M_STT);
  LAS float* CS = (LAS float*)(L + M_CS);
  u64 tag;

  if (FFN) {
    tag = *(const CAS u64*)(ws + WS_EP) + 1ull;
    if (tid < 16) CNT[tid] = 0u;
    v4u xv[2][4];
    unsigned pv[3] = {0u, 0u, 0u};
    if (tg == 3) xload8(xv, (const char*)(a.x + (size_t)(128 * r + 8 * c + r8) * D_ + p8 * 8));
    if (tg == 2) pload3(pv, (32 * c + (lane & 31)) * 2, a.Lre, a.Lim, a.logdt);
    BAR();
    waitv<0>();
    if (tg == 2) {
      asm volatile("" : "+v"(pv[0]), "+v"(pv[1]), "+v"(pv[2]));
      const float lr = bf2f(pv[0]), li = bf2f(pv[1]), dt = rbf(expf(bf2f(pv[2])));
      const float er = expf(dt * lr); float sn, cn; sincosf(dt * li, &sn, &cn);
      const float br = er * cn, bi = er * sn, den = 1.f / (lr * lr + li * li), nr = br - 1.f;
      if (lane < 32) *(LAS f32x4*)(L + M_PRM + lane * 16) = f32x4{br, bi, (nr * lr + bi * li) * den, (bi * lr - nr * li) * den};
    }
    if (tg == 3) {
      pin4(xv[0]); pin4(xv[1]);
      f32x2 sv = {0.f, 0.f};
#pragma unroll
      for (int j = 0; j < 8; j++)
#pragma unroll
        for (int e = 0; e < 4; e++) sv += f32x2{bf2f(xv[j >> 2][j & 3][e]), bfhi(xv[j >> 2][j & 3][e])};
      float sm = sv.x + sv.y;
      sm += __shfl_xor(sm, 1); sm += __shfl_xor(sm, 2); sm += __shfl_xor(sm, 4);
      const float m = sm * (1.f / 512.f);
      f32x2 qv = {0.f, 0.f};
#pragma unroll
      for (int j = 0; j < 8; j++)
#pragma unroll
        for (int e = 0; e < 4; e++) { const f32x2 d = f32x2{bf2f(xv[j >> 2][j & 3][e]), bfhi(xv[j >> 2][j & 3][e])} - m; qv = __builtin_elementwise_fma(d, d, qv); }
      float sq = qv.x + qv.y;
      sq += __shfl_xor(sq, 1); sq += __shfl_xor(sq, 2); sq += __shfl_xor(sq, 4);
      const float sd = __builtin_amdgcn_sqrtf(rbf(rbf(sq * (1.f / 512.f)) + 1e-5f));
      if (p8 == 0) __builtin_amdgcn_raw_buffer_store_b32(pkbf(m, sd), rsw, (int)WS_ST + (128 * r + 8 * c + r8) * 4, 0, AUX_SC1);
    }
    unsigned wlo[7], whi[7];
#pragma unroll
    for (int v = 0; v < 7; v++) {
      const int pi = codd ? (v == 6 ? 1 : v + 2) : v;
      wlo[v] = lb + OFF_W + (8 * g + q) * 256 + ((pi ^ fl) << 5) + p4 * 8; whi[v] = wlo[v] + 2 * SLOT;
    }
    f32x4 acc[7][2];
#pragma unroll
    for (int u = 0; u < 7; u++) { acc[u][0] = f32x4{0, 0, 0, 0}; acc[u][1] = f32x4{0, 0, 0, 0}; }
    v4u xf[2][2]; Tr14 wt[2];
    auto step = [&](auto kc) {
      constexpr int ks = decltype(kc)::value, kk = ks & 1, kn = ks + 1, nk = kn & 1, sl = (kn >> 1) & 3;
      static_for<14>([&](auto mc) {
        constexpr int m = decltype(mc)::value, v = m >> 1, tt = m & 1;
        if constexpr (ks >= 0) {
          const v4u wv = {wt[kk].h[2 * v].x, wt[kk].h[2 * v].y, wt[kk].h[2 * v + 1].x, wt[kk].h[2 * v + 1].y};
          acc[v][tt] = mfma(wv, xf[kk][tt], acc[v][tt]);
          SB0();
        }
        if constexpr (kn < 16) {
          if (m == 0) rd2x<(sl & 1) * SLOT>(xf[nk], xad[(sl >> 1) * 2 + nk]);
          if constexpr (m >= 1 && m <= 7) tr2<(sl & 1) * SLOT + nk * 8192>(wt[nk].h[2 * (m - 1)], wt[nk].h[2 * (m - 1) + 1], (sl >> 1) ? whi[m - 1] : wlo[m - 1]);
          SB0();
        }
      });
      if constexpr (ks == 0 || ks == 1) {
        if (tg == 0 && ks == (r >> 3)) {
#pragma unroll
          for (int v = 0; v < 7; v++) {
            const f32x4 cp = mfma(v4u{wt[kk].h[2 * v].x, wt[kk].h[2 * v].y, wt[kk].h[2 * v + 1].x, wt[kk].h[2 * v + 1].y}, ones, f32x4{0, 0, 0, 0});
            if (tok == 0) __builtin_amdgcn_raw_buffer_store_b128(__builtin_bit_cast(v4u, cp), rsw, (int)WS_CP + (c * 16 + r) * 448 + v * 64 + g * 16, 0, AUX_SC1);
          }
        }
      }
      if constexpr (ks >= 0) keep(wt[kk], xf[kk]);
      if constexpr (kn < 16) waitl_pin<0>(wt[nk], xf[nk]);
    };
    asm volatile("" :: "s"(tag));
    WAITL0(); BAR();
    ST(1);
    step(IC(-1));
    v2u ef[4]; v4u gc[8]; v2u gs;
    static_for<8>([&](auto sc) {
      constexpr int s = decltype(sc)::value;
      step(IC(2 * s));
      if (s == 1 && (tg == 0 || tg == 3)) {
        waitv<0>();
        if (lds_arrive(CNT + 5) == 1u && lane == 0) __hip_atomic_store((GAS u64*)(ws + WS_EF + b * 8), tag, RLX_AGENT);
      }
      if (s == 7 && tg == 1) {
        efwait(ef);
        bool ok = true;
#pragma unroll
        for (int j = 0; j < 4; j++) ok = ok && ef[j].x == (unsigned)tag && ef[j].y == (unsigned)(tag >> 32);
        if (!__all(ok)) {
#pragma unroll
          for (int j = 0; j < 4; j++) poll_flags(ws + WS_EG + j * 512, 64, tag);
        }
        if (b == 0 && lane == 0) __hip_atomic_store((GAS u64*)(ws + WS_EP), tag, RLX_AGENT);
      }
      if (s == 5 && tg == 0) {
        efwait(ef);
        bool ok = true;
#pragma unroll
        for (int j = 0; j < 4; j++) ok = ok && ef[j].x == (unsigned)tag && ef[j].y == (unsigned)(tag >> 32);
        if (!__all(ok)) {
#pragma unroll
          for (int j = 0; j < 4; j++) poll_flags(ws + WS_EF + j * 512, 64, tag);
        }
        __builtin_amdgcn_fence(__ATOMIC_ACQUIRE, "wavefront");
        const int hl = lane >= 28 ? lane - 28 : lane, hr = lane >= 28 ? 8 : 0;
#pragma unroll
        for (int j = 0; j < 8; j++) gc[j] = __builtin_amdgcn_raw_buffer_load_b128(rsw, (int)WS_CP + (c * 16 + hr + j) * 448 + (hl < 28 ? hl : 0) * 16, 0, AUX_SC1);
        gs = __builtin_amdgcn_raw_buffer_load_b64(rsw, (int)WS_ST + (128 * r + 2 * lane) * 4, 0, AUX_SC1);
      }
      BAR();
      if (s == 3) ST(2);
      if (s == 3 && tg == 0) efload(ef, ws + WS_EF + lane * 8);
      if (s == 5 && tg == 1) efload(ef, ws + WS_EG + lane * 8);
      step(IC(2 * s + 1));
    });
    if (tg == 0) {
      f32x4 t = __builtin_bit_cast(f32x4, gc[0]);
#pragma unroll
      for (int j = 1; j < 8; j++) t += __builtin_bit_cast(f32x4, gc[j]);
      const f32x4 o = {__shfl_down(t[0], 28), __shfl_down(t[1], 28), __shfl_down(t[2], 28), __shfl_down(t[3], 28)};
      if (lane < 28) *(LAS f32x4*)(CS + lane * 4) = t + o;
      *(LAS f32x4*)(STT + 2 * lane) = f32x4{bf2f(gs.x), fdiv(1.f, bfhi(gs.x)), bf2f(gs.y), fdiv(1.f, bfhi(gs.y))};
    }
    ST(3);
    WAITL0(); BAR();
    ST(4);
    {
      const f32x2 st0 = STT[32 * tg + tok], st1 = STT[32 * tg + 16 + tok];
      f32x4 cs[7];
#pragma unroll
      for (int v = 0; v < 7; v++) cs[v] = *(LAS f32x4*)(CS + v * 16 + 4 * g);
      const int T16 = 8 * r + 2 * tg;
      const int fb = (int)WS_H + ((T16 >> 2) * KS2 * 4 + (T16 & 3)) * 1024 + lane * 16;
      const float km[2] = {-st0.x * st0.y, -st1.x * st1.y}, rs[2] = {st0.y, st1.y};
      v2u o[2][2];
#pragma unroll
      for (int v = 0; v < 7; v++) {
#pragma unroll
        for (int tt = 0; tt < 2; tt++) {
          unsigned pk[2];
#pragma unroll
          for (int hh = 0; hh < 2; hh++) {
            const f32x2 ac = {acc[v][tt][2 * hh], acc[v][tt][2 * hh + 1]}, cc = {cs[v][2 * hh], cs[v][2 * hh + 1]};
            const f32x2 u = __builtin_elementwise_fma(ac, f32x2{rs[tt], rs[tt]}, cc * km[tt]);
            unsigned hb = pkbf(u.x, u.y);
            asm volatile("" : "+v"(hb));
            pk[hh] = silu2(hb);
          }
          o[v & 1][tt] = v2u{pk[0], pk[1]};
        }
        if (v & 1) {
          const int U = 7 * c + v - 1 + codd;
#pragma unroll
          for (int tt = 0; tt < 2; tt++)
            __builtin_amdgcn_raw_buffer_store_b128(v4u{o[0][tt].x, o[0][tt].y, o[1][tt].x, o[1][tt].y}, rsw, fb + tt * 1024 + (U >> 1) * 4096, 0, AUX_SC1);
        } else if (v == 6) {
          const int U = codd ? 7 * c : 7 * c + 6;
#pragma unroll
          for (int tt = 0; tt < 2; tt++)
            __builtin_amdgcn_raw_buffer_store_b64(o[0][tt], rsw, fb + tt * 1024 + (U >> 1) * 4096 + (U & 1) * 8, 0, AUX_SC1);
        }
      }
    }
    ST(5);
    lds_arrive(CNT + 7);
    waitv<0>();
    if (lds_arrive(CNT + 3) == 3u && lane == 0) __hip_atomic_store((GAS u64*)(ws + WS_FL + r * 256 + c * 8), tag, RLX_AGENT);
    ST(6);
  } else {
    unsigned sx[2], sw[2];
    const char* ssp[2];
#pragma unroll
    for (int h = 0; h < 2; h++) {
      sx[h] = ((8 * h + r8) * D_ + ((p8 ^ ((4 * h + (r8 >> 1)) & 7)) << 3)) * 2;
      sw[h] = ((8 * h + g) * F_ + (((((tok >> 1) ^ (g | (h << 2))) << 1) | (tok & 1)) << 3)) * 2;
      const int n = 16 * tg + 8 * h + r8;
      ssp[h] = (const char*)(((n & 1) ? a.Bim : a.Bre) + (size_t)(32 * c + (n >> 1)) * D_ + ((p8 ^ ((n >> 1) & 7)) << 3));
    }
    const char* xb = (const char*)(a.x + (size_t)(128 * r + 32 * tg) * D_);
    const char* wb1 = (const char*)(a.w1 + (size_t)(16 * tg) * F_ + 112 * c - 16 * codd);
    const int rot = r & 7;
    auto issue1 = [&](int s, int j) {
      LAS char* sb = L + (s & 3) * SLOT;
      const int k0 = ((s + rot) & 7) * 64;
      if (j < 4) glds16(xb + ((j >> 1) * 16 * D_ + k0) * 2 + sx[j & 1], sb + (4 * tg + j) * 1024);
      else glds16(wb1 + (size_t)(k0 + (j & 1) * 4) * F_ * 2 + sw[(j - 4) >> 1], sb + OFF_W + (4 * tg + j - 4) * 1024);
    };
    BAR();
#pragma unroll
    for (int s = 0; s < 2; s++)
#pragma unroll
      for (int j = 0; j < 8; j++) issue1(s, j);
    __builtin_amdgcn_s_setprio(3);
    tag = *(const CAS u64*)(ws + WS_EP) + 1ull;
    auto piece = [&](int s, int j) {
      if (s <= 3) issue1(s + 4, j);
      else if (s <= 5) glds16(ssp[j & 1] + ((4 * (s - 4) + (j >> 1) + rot) & 7) * 128, L + (s - 4) * SLOT + (j >> 1) * 8192 + (2 * tg + (j & 1)) * 1024);
    };
    const int blk = b + 256 * tg, bcb = blk / KS2, bks = blk - bcb * KS2;
    const bool doblk = tg < 3 && blk < 8 * KS2;
    LAS char* esc = L + M_TR + tg * 4096;
    v4u xr[16][2];
    auto take = [&](auto kc, int sd, int j0, int nj) {
      constexpr int ks = decltype(kc)::value, kk = ks & 1, sl = (ks >> 1) & 3;
      rd2x<(sl & 1) * SLOT>(xr[ks], xad[(sl >> 1) * 2 + kk]);
#pragma unroll
      for (int j = 0; j < nj; j++) piece(sd, j0 + j);
      waitl_pin2(xr[ks]);
    };
    const int sch = lane & 7, seg = lane >> 3, chg = 32 * c + 8 * tg + sch;
    cplx lam, coef, l16, l32, l64, lseg = {1.f, 0.f}, wa = {0.f, 0.f}, wb = {0.f, 0.f};
    asm volatile("" :: "s"(tag));
    waitv<8>(); BAR();
    ST(1);
    take(IC(0), -2, 0, 8);
    static_for<8>([&](auto sc) {
      constexpr int s = decltype(sc)::value;
      if (s == 0) {
        take(IC(1), -1, 0, 8);
        if (doblk) {
          if (bks < 56) {
            const char* src = (const char*)(a.w2 + (size_t)(32 * bks + r8) * D_ + 64 * bcb + (((((p8 >> 1) ^ ((r8 >> 1) & 3)) << 1) | (p8 & 1)) << 3));
#pragma unroll
            for (int j = 0; j < 4; j++) glds16(src + j * 8 * D_ * 2, esc + j * 1024);
          } else {
            const int r4 = lane >> 2;
            const char* src = (const char*)((((bks - 56) & 1) ? a.Cim : a.Cre) + (size_t)(64 * bcb + r4) * 512 + 32 * ((bks - 56) >> 1) + ((p4 ^ ((-(r4 >> 2)) & 3)) << 3));
#pragma unroll
            for (int j = 0; j < 4; j++) glds16(src + j * 16 * 1024, esc + j * 1024);
          }
        }
      } else take(IC(2 * s + 1), s - 1, 4, 4);
      if (s <= 5) waitv<16>();
      if (s == 6) waitv<16>();
      if (s == 2 && doblk) {
        v4u wf[4];
        if (bks < 56) {
          const int fw = (2 * g + (q >> 1)) & 3;
          const unsigned tb = (unsigned)(size_t)esc + (4 * g + q) * 128 + p4 * 8;
          trread8(wf, tb + ((0 ^ fw) << 5), tb + ((1 ^ fw) << 5), tb + ((2 ^ fw) << 5), tb + ((3 ^ fw) << 5));
        } else rd4k(wf, (unsigned)(size_t)esc + tok * 64 + ((g ^ ((-(tok >> 2)) & 3)) << 4));
#pragma unroll
        for (int j = 0; j < 4; j++) __builtin_amdgcn_raw_buffer_store_b128(wf[j], rsw, (int)WS_BT + blk * 4096 + j * 1024 + lane * 16, 0, AUX_SC1);
      }
      if (s == 4) {
        if (lds_arrive(CNT + 8) == 3u && lane == 0) __hip_atomic_store((GAS u64*)(ws + WS_EG + b * 8), tag, RLX_AGENT);
      }
      BAR();
      if (s == 3) ST(2);
      if constexpr (s < 7) take(IC(2 * s + 2), s, 0, 4);
      if (s == 1) {
        const f32x4 pr = *(LAS f32x4*)(L + M_PRM + (8 * tg + sch) * 16);
        lam = {pr[0], pr[1]}; coef = {pr[2], pr[3]};
        const cplx l2 = cmul(lam, lam), l4 = cmul(l2, l2), l8 = cmul(l4, l4);
        l16 = cmul(l8, l8); l32 = cmul(l16, l16); l64 = cmul(l32, l32);
        asm volatile("" : "+v"(l64.r), "+v"(l64.i));
      }
      if (s == 3) {
        const cplx l128 = cmul(l64, l64), l256 = cmul(l128, l128), l512 = cmul(l256, l256), l1024 = cmul(l512, l512);
        if (seg & 1) lseg = l16;
        if (seg & 2) lseg = cmul(lseg, l32);
        if (seg & 4) lseg = cmul(lseg, l64);
        const int nb = r - 1 - (seg + 8);
        cplx t = {1.f, 0.f};
        const int ne = nb >= 0 ? nb : (nb + 8 >= 0 ? nb + 8 : 0);
        if (ne & 1) t = l128;
        if (ne & 2) t = cmul(t, l256);
        if (ne & 4) t = cmul(t, l512);
        if (nb >= 0) { wb = t; wa = cmul(t, l1024); } else if (nb + 8 >= 0) { wa = t; }
        asm volatile("" : "+v"(wa.r), "+v"(wa.i), "+v"(wb.r), "+v"(wb.i), "+v"(lseg.r), "+v"(lseg.i));
      }
    });
    ST(3);
    BAR();
    ST(4);
    __builtin_amdgcn_s_setprio(0);
#pragma unroll
    for (int j = 0; j < 16; j++) {
      const int o = 16 * tg + j;
      const int ks = o < 48 ? 14 * (o / 12) + ((o % 12) >> 2) : 56 + 8 * ((o - 48) >> 2);
      glds16c(bt + ks * 4096 + (o & 3) * 1024 + lane * 16, L + 65536 + o * 1024);
    }
    f32x4 acc[4][2], cs4 = {0, 0, 0, 0};
#pragma unroll
    for (int u = 0; u < 4; u++) { acc[u][0] = f32x4{0, 0, 0, 0}; acc[u][1] = f32x4{0, 0, 0, 0}; }
    const unsigned sad[2] = {lb + tok * 128 + ((g ^ swz) << 4), lb + ((tok * 128 + ((g ^ swz) << 4)) ^ 64)};
    v4u wf[3][4], wo[3];
    const unsigned oad[2] = {sad[0] + tg * 2048, sad[1] + tg * 2048};
    waitv<0>();
    lds_arrive(CNT + 0); lds_wait(CNT + 0, 4u);
    rd4w<0>(wf[0], sad[0]); rd1<0>(wo[0], oad[0]);
    rd4w<0>(wf[1], sad[1]); rd1<0>(wo[1], oad[1]);
    lds_wait(CNT + 7, 4u);
    static_for<16>([&](auto kc) {
      constexpr int ks = decltype(kc)::value, kn = ks + 2;
      if constexpr (kn < 16) {
        rd4w<(kn >> 3) * SLOT + ((kn >> 1) & 3) * 8192>(wf[kn % 3], sad[kn & 1]);
        rd1<(kn >> 3) * SLOT + ((kn >> 1) & 3) * 8192>(wo[kn % 3], oad[kn & 1]);
        waitl_pin5<10>(wf[ks % 3], wo[ks % 3]);
      } else if constexpr (kn == 16) waitl_pin5<5>(wf[ks % 3], wo[ks % 3]);
      else waitl_pin5<0>(wf[ks % 3], wo[ks % 3]);
      SB0();
#pragma unroll
      for (int u = 0; u < 4; u++) { acc[u][0] = mfma(wf[ks % 3][u], xr[ks][0], acc[u][0]); acc[u][1] = mfma(wf[ks % 3][u], xr[ks][1], acc[u][1]); }
      cs4 = mfma(wo[ks % 3], ones, cs4);
      if (ks == 8) lds_arrive(CNT + 1);
      SB0();
    });
    ST(5);
    __builtin_amdgcn_s_setprio(3);
    lds_wait(CNT + 1, 4u);
#pragma unroll
    for (int u = 0; u < 4; u++)
#pragma unroll
      for (int tt = 0; tt < 2; tt++) {
        const int token = 32 * tg + 16 * tt + tok;
        *(LAS f32x4*)(L + token * 256 + (((4 * u + g) ^ (token & 7) ^ (((token >> 4) & 3) << 2)) << 4)) = acc[u][tt];
      }
    const float ca = lane & 1 ? cs4[2] : cs4[0], cb = lane & 1 ? cs4[3] : cs4[1];
    const f32x2 c2 = {__shfl(ca, (sch >> 1) * 16 + (sch & 1)), __shfl(cb, (sch >> 1) * 16 + (sch & 1))};
    lds_arrive(CNT + 2);
    lds_wait(CNT + 2, 4u);
    cplx bu[16], h = {0.f, 0.f}, init0 = {0.f, 0.f};
    {
      const int rbase = seg * 4096 + (sch & 1) * 8, rch = (4 * tg + (sch >> 1)) ^ ((seg & 3) << 2);
      f32x2 v[16]; f32x4 st[8];
#pragma unroll
      for (int t = 0; t < 16; t++) v[t] = *(LAS f32x2*)(L + rbase + t * 256 + ((rch ^ (t & 7)) << 4));
#pragma unroll
      for (int t = 0; t < 8; t++) st[t] = *(LAS f32x4*)(L + M_STT + (16 * seg + 2 * t) * 8);
#pragma unroll
      for (int t = 0; t < 16; t++) {
        const f32x2 uu = (v[t] - st[t >> 1][(t & 1) * 2] * c2) * st[t >> 1][(t & 1) * 2 + 1];
        bu[t] = {uu.x, uu.y};
        h = cfma(lam, h, bu[t]);
      }
      { cplx o = {__shfl_up(h.r, 8), __shfl_up(h.i, 8)}; if (seg >= 1) h = cfma(l16, o, h); }
      { cplx o = {__shfl_up(h.r, 16), __shfl_up(h.i, 16)}; if (seg >= 2) h = cfma(l32, o, h); }
      { cplx o = {__shfl_up(h.r, 32), __shfl_up(h.i, 32)}; if (seg >= 4) h = cfma(l64, o, h); }
      if (seg == 7 && r < 15) {
        const unsigned ur = __builtin_bit_cast(unsigned, h.r), ui = __builtin_bit_cast(unsigned, h.i);
        GAS u64* gp = (GAS u64*)(ws + WS_E + ((size_t)r * 512 + chg) * 16);
        __hip_atomic_store(gp, ((u64)((unsigned)tag + hashA(ur, ui)) << 32) | ur, RLX_AGENT);
        __hip_atomic_store(gp + 1, ((u64)((unsigned)(tag >> 32) + hashB(ur, ui)) << 32) | ui, RLX_AGENT);
      }
      { cplx o = {__shfl_up(h.r, 8), __shfl_up(h.i, 8)}; if (seg >= 1) init0 = o; }
    }
    ST(6);
    cplx carry = {0.f, 0.f};
    if (r > 0) {
      const bool needA = seg < r, needB = seg + 8 < r;
      GAS u64* ga = (GAS u64*)(ws + WS_E + ((size_t)seg * 512 + chg) * 16);
      GAS u64* gb = ga + (size_t)8 * 512 * 2;
      u64 a0 = 0, a1 = 0, b0 = 0, b1 = 0;
      for (unsigned spins = 0; spins < SPIN_CAP; ++spins) {
        bool ok = true;
        if (needA) { a0 = __hip_atomic_load(ga, RLX_AGENT); a1 = __hip_atomic_load(ga + 1, RLX_AGENT); }
        if (needB) { b0 = __hip_atomic_load(gb, RLX_AGENT); b1 = __hip_atomic_load(gb + 1, RLX_AGENT); }
        if (needA) ok = (unsigned)(a0 >> 32) == (unsigned)tag + hashA((unsigned)a0, (unsigned)a1) &&
                        (unsigned)(a1 >> 32) == (unsigned)(tag >> 32) + hashB((unsigned)a0, (unsigned)a1);
        if (needB) ok = ok && (unsigned)(b0 >> 32) == (unsigned)tag + hashA((unsigned)b0, (unsigned)b1) &&
                        (unsigned)(b1 >> 32) == (unsigned)(tag >> 32) + hashB((unsigned)b0, (unsigned)b1);
        if (__all(ok)) break;
        __builtin_amdgcn_s_sleep(3);
      }
      const cplx ea = {__builtin_bit_cast(float, (unsigned)a0), __builtin_bit_cast(float, (unsigned)a1)};
      const cplx eb = {__builtin_bit_cast(float, (unsigned)b0), __builtin_bit_cast(float, (unsigned)b1)};
      cplx s1 = {0.f, 0.f};
      if (needA) s1 = cmul(wa, ea);
      if (needB) s1 = cfma(wb, eb, s1);
      s1.r += __shfl_xor(s1.r, 8); s1.i += __shfl_xor(s1.i, 8);
      s1.r += __shfl_xor(s1.r, 16); s1.i += __shfl_xor(s1.i, 16);
      s1.r += __shfl_xor(s1.r, 32); s1.i += __shfl_xor(s1.i, 32);
      carry = s1;
    }
    ST(7);
    h = cfma(lseg, carry, init0);
    LAS char* tr = L + M_TR + tg * 4224;
#pragma unroll
    for (int t = 0; t < 16; t++) {
      h = cfma(lam, h, bu[t]);
      const cplx o = cmul(coef, h);
      const unsigned pk = pkbf(o.r, -o.i);
      LAS u16* dst = (LAS u16*)(tr + seg * 528 + t * 32 + sch * 2);
      dst[0] = (u16)pk; dst[8] = (u16)(pk >> 16);
    }
    CBAR();
#pragma unroll
    for (int j = 0; j < 4; j++) {
      const int part = j & 1, token = (j >> 1) * 64 + lane;
      const v4u v = *(LAS v4u*)(tr + (token >> 4) * 528 + (token & 15) * 32 + part * 16);
      const int T16 = 8 * r + (token >> 4);
      __builtin_amdgcn_raw_buffer_store_b128(v, rsw, (int)WS_H + (((T16 >> 2) * KS2 + 56 + 2 * c + part) * 4 + (T16 & 3)) * 1024 + ((token & 15) + 16 * tg) * 16, 0, AUX_SC1);
    }
    waitv<0>();
    if (lds_arrive(CNT + 4) == 3u && lane == 0) __hip_atomic_store((GAS u64*)(ws + WS_FL + r * 256 + 128 + c * 8), tag, RLX_AGENT);
    __builtin_amdgcn_s_setprio(1);
  }
  ST(8);
  const char* bp = bt + (size_t)s0 * 4096;
  v4u rb[NRB][4];
#pragma unroll
  for (int i = 0; i < NRB; i++) aload4(rb[i], lane * 16, bp + (size_t)(NL + i) * 4096);
  v2u ex[2]; f32x4 sk[2];
  {
    v2u ed[2]; unsigned es[2];
#pragma unroll
    for (int j = 0; j < 2; j++) {
      const int it = tid + 512 * j, token = 64 * rt + (it >> 4), d = 64 * ct + 4 * (it & 15);
      ex[j] = *(const v2u*)(a.x + (size_t)token * D_ + d);
      ed[j] = *(const v2u*)(a.Dsk + d);
      es[j] = __builtin_amdgcn_raw_buffer_load_b32(rsw, (int)WS_ST + token * 4, 0, AUX_SC1);
    }
#pragma unroll
    for (int j = 0; j < 2; j++)
#pragma unroll
      for (int e = 0; e < 4; e++) {
        const unsigned xw = e < 2 ? ex[j].x : ex[j].y, dw = e < 2 ? ed[j].x : ed[j].y;
        const float xv = (e & 1) ? bfhi(xw) : bf2f(xw), dv = (e & 1) ? bfhi(dw) : bf2f(dw);
        sk[j][e] = rbf(rbf(fdiv(rbf(xv - bf2f(es[j])), bfhi(es[j]))) * dv);
      }
    asm volatile("" : "+v"(sk[0]), "+v"(sk[1]));
  }
  if (FFN) {
    if (tg == 0) { poll_flags(ws + WS_FL + (rt >> 1) * 256, 16, tag); lds_arrive(CNT + 6); }
    else lds_wait(CNT + 6, 1u);
  } else poll_flags(ws + WS_FL + (rt >> 1) * 256 + 128 + tg * 32, 4, tag);
  ST(9);
  const char* ap = ws + WS_H + ((size_t)rt * KS2 + s0) * 4096;
  v4u ra[WA][4];
#pragma unroll
  for (int i = 0; i < WA; i++) aload4(ra[i], lane * 16, ap + (size_t)i * 4096);
  if (FFN) lds_wait(CNT + 0, 4u);
  f32x4 acc2[4][4];
#pragma unroll
  for (int i = 0; i < 4; i++)
#pragma unroll
    for (int j = 0; j < 4; j++) acc2[i][j] = f32x4{0, 0, 0, 0};
  static_for<N2>([&](auto ic) {
    constexpr int i = decltype(ic)::value;
    constexpr bool inreg = i >= NL && i < NL + NRB;
    waitv<p2_wait(i, N2, WA, FFN)>();
    pin4(ra[i % WA]);
    if (i == 0) ST(10);
    if (i == 4) ST(11);
    v4u wf[4];
    if constexpr (inreg) pin4(rb[i - NL]);
    else rd4k(wf, (unsigned)(size_t)ringp(i) + lane * 16);
#pragma unroll
    for (int j = 0; j < 4; j++)
#pragma unroll
      for (int t = 0; t < 4; t++) acc2[j][t] = mfma(inreg ? rb[inreg ? i - NL : 0][j] : wf[j], ra[i % WA][t], acc2[j][t]);
    SB0();
    if constexpr (p2_refill(i, FFN) >= 0) {
      LAS char* sl = ringp(p2_refill(i, FFN));
#pragma unroll
      for (int j = 0; j < 4; j++) glds16c(bp + (size_t)p2_refill(i, FFN) * 4096 + j * 1024 + lane * 16, sl + j * 1024);
    }
    if (i + WA < N2) aload4(ra[i % WA], lane * 16, ap + (size_t)(i + WA) * 4096);
    SB0();
  });
  ST(12);
  BAR();
#pragma unroll
  for (int t = 0; t < 4; t++)
#pragma unroll
    for (int j = 0; j < 4; j++)
      *(LAS f32x4*)(L + t * SLOT + w * 4096 + (j * 4 + g) * 256 + ((tok + 4 * j + g) & 15) * 16) = acc2[j][t];
  WAITL0(); BAR();
  ST(13);
#pragma unroll
  for (int j = 0; j < 2; j++) {
    const int it = tid + 512 * j, tl = it >> 4, dq = it & 15;
    LAS char* pb = L + (tl >> 4) * SLOT + dq * 256 + (((tl & 15) + dq) & 15) * 16;
    const f32x4 ff = (*(LAS f32x4*)(pb) + *(LAS f32x4*)(pb + 4096)) + (*(LAS f32x4*)(pb + 8192) + *(LAS f32x4*)(pb + 12288));
    const f32x4 sy = (*(LAS f32x4*)(pb + 16384) + *(LAS f32x4*)(pb + 20480)) + (*(LAS f32x4*)(pb + 24576) + *(LAS f32x4*)(pb + 28672));
    unsigned g01 = pkbf(ff[0], ff[1]), g23 = pkbf(ff[2], ff[3]);
    asm volatile("" : "+v"(g01), "+v"(g23));
    g01 = silu2(g01); g23 = silu2(g23);
    const f32x4 gg = {bf2f(g01), bfhi(g01), bf2f(g23), bfhi(g23)};
    const f32x4 xv = {bf2f(ex[j].x), bfhi(ex[j].x), bf2f(ex[j].y), bfhi(ex[j].y)};
    *(f32x4*)(a.out + (size_t)(64 * rt + tl) * D_ + 64 * ct + 4 * dq) = xv + (2.f * sy + sk[j]) * gg;
  }
  ST(14);
  ST_FLUSH(ws);
}

__global__ __launch_bounds__(512, 2) void k_fused(Args a) {
  extern __shared__ __attribute__((aligned(16))) char smem[];
  LAS char* L = (LAS char*)smem;
  const int w = __builtin_amdgcn_readfirstlane(threadIdx.x >> 6);
  if (w < 4) body<true>(a, L, w);
  else body<false>(a, L, w);
}
}

extern "C" void kernel_launch(void* const* d_in, const int* in_sizes, int n_in, void* d_out, int out_size, void* d_ws, size_t ws_size,
                              hipStream_t stream) {
  Args a;
  a.x = (const u16*)d_in[0]; a.Lre = (const u16*)d_in[1]; a.Lim = (const u16*)d_in[2];
  a.Bre = (const u16*)d_in[3]; a.Bim = (const u16*)d_in[4]; a.Cre = (const u16*)d_in[5]; a.Cim = (const u16*)d_in[6];
  a.logdt = (const u16*)d_in[7]; a.Dsk = (const u16*)d_in[8]; a.w1 = (const u16*)d_in[10]; a.w2 = (const u16*)d_in[11];
  a.ws = (char*)d_ws; a.out = (float*)d_out;
  (void)hipFuncSetAttribute((const void*)k_fused, hipFuncAttributeMaxDynamicSharedMemorySize, LDS_BYTES);
  k_fused<<<256, 512, LDS_BYTES, stream>>>(a);
}
```

```cpp
#include <hip/hip_runtime.h>
#include <hip/hip_bf16.h>
#include <stdint.h>
#include <utility>

typedef unsigned short u16;
typedef unsigned long long u64;
typedef unsigned v4u __attribute__((ext_vector_type(4)));
typedef unsigned v2u __attribute__((ext_vector_type(2)));
typedef float f32x4 __attribute__((ext_vector_type(4)));
typedef float f32x2 __attribute__((ext_vector_type(2)));
typedef short bf16x8 __attribute__((ext_vector_type(8)));
typedef short v4s __attribute__((ext_vector_type(4)));
typedef __bf16 v2bf __attribute__((ext_vector_type(2)));
#define LAS __attribute__((address_space(3)))
#define GAS __attribute__((address_space(1)))
#define RLX_AGENT __ATOMIC_RELAXED, __HIP_MEMORY_SCOPE_AGENT
#define RLX_WG __ATOMIC_RELAXED, __HIP_MEMORY_SCOPE_WORKGROUP

namespace {
constexpr int D_ = 512, F_ = 1792;
constexpr int KS2 = 88;
constexpr size_t WS_H = 0;
constexpr size_t WS_H_BYTES = (size_t)32 * KS2 * 4096;
constexpr size_t WS_ST = WS_H + WS_H_BYTES;
constexpr size_t WS_E = WS_ST + 2048 * 8;
constexpr size_t WS_FL = WS_E + 16 * 512 * 16;
constexpr size_t WS_EP = WS_FL + 16 * 256;
constexpr size_t WS_END = WS_EP + 128;
constexpr int SLOT = 32768, OFF_W = 16384;
constexpr int M_STT = 131072, M_CS = M_STT + 1024, M_PRM = M_CS + 512, M_ST2 = M_PRM + 512, M_CNT = M_ST2 + 512, M_TR = M_CNT + 256;
constexpr int LDS_BYTES = 163840;
constexpr unsigned SPIN_CAP = 1u << 17;
#define FAUX 16
constexpr int AUX_SC1 = 16;
#define PSLEEP 6

__device__ __forceinline__ float bf2f(unsigned b) { return __builtin_bit_cast(float, b << 16); }
__device__ __forceinline__ float bfhi(unsigned b) { return __builtin_bit_cast(float, b & 0xffff0000u); }
__device__ __forceinline__ unsigned pkbf(float lo, float hi) {
  v2bf v; v.x = (__bf16)lo; v.y = (__bf16)hi; return __builtin_bit_cast(unsigned, v);
}
__device__ __forceinline__ float rbf(float f) { return bf2f(pkbf(f, 0.f)); }
__device__ __forceinline__ f32x4 mfma(v4u a, v4u b, f32x4 c) {
  return __builtin_amdgcn_mfma_f32_16x16x32_bf16(__builtin_bit_cast(bf16x8, a), __builtin_bit_cast(bf16x8, b), c, 0, 0, 0);
}
__device__ __forceinline__ float dot2(unsigned a, unsigned b, float c) {
  return __builtin_amdgcn_fdot2_f32_bf16(__builtin_bit_cast(v2bf, a), __builtin_bit_cast(v2bf, b), c, false);
}
__device__ __forceinline__ void glds16(const void* g, LAS char* l) {
  __builtin_amdgcn_global_load_lds((const GAS void*)g, (LAS void*)l, 16, 0, 0);
}
__device__ __forceinline__ v2u trread(LAS char* p) {
  return __builtin_bit_cast(v2u, __builtin_amdgcn_ds_read_tr16_b64_v4i16((LAS v4s*)p));
}
__device__ __forceinline__ float silu_f(float h) { return h * __builtin_amdgcn_rcpf(1.f + __builtin_amdgcn_exp2f(h * -1.44269504088896f)); }
__device__ __forceinline__ unsigned silu2(unsigned hb) {
  const f32x2 h = {bf2f(hb), bfhi(hb)};
  const f32x2 t = h * -1.44269504088896f;
  const f32x2 d = f32x2{__builtin_amdgcn_exp2f(t.x), __builtin_amdgcn_exp2f(t.y)} + 1.f;
  const f32x2 y = h * f32x2{__builtin_amdgcn_rcpf(d.x), __builtin_amdgcn_rcpf(d.y)};
  return pkbf(y.x, y.y);
}
struct cplx { float r, i; };
__device__ __forceinline__ cplx cmul(cplx a, cplx b) { return {__builtin_fmaf(a.r, b.r, -a.i * b.i), __builtin_fmaf(a.r, b.i, a.i * b.r)}; }
__device__ __forceinline__ cplx cfma(cplx a, cplx b, cplx c) {
  return {__builtin_fmaf(a.r, b.r, __builtin_fmaf(-a.i, b.i, c.r)), __builtin_fmaf(a.r, b.i, __builtin_fmaf(a.i, b.r, c.i))};
}
__device__ __forceinline__ unsigned hashA(unsigned a, unsigned b) { unsigned h = a * 0x9E3779B1u + (b ^ (b >> 15)) * 0x85EBCA77u; return h ^ (h >> 13); }
__device__ __forceinline__ unsigned hashB(unsigned a, unsigned b) { unsigned h = b * 0xC2B2AE3Du + (a ^ (a >> 16)) * 0x27D4EB2Fu; return h ^ (h >> 15); }
template <int N> __device__ __forceinline__ void waitv() { asm volatile("s_waitcnt vmcnt(%0)" ::"n"(N) : "memory"); }
#define WAITL0() __builtin_amdgcn_s_waitcnt(0xC07F)
#define BAR() __builtin_amdgcn_s_barrier()
#define SB0() __builtin_amdgcn_sched_barrier(0)
#define CBAR() asm volatile("" ::: "memory")
#define ST_DECL
#define ST(i)
#define SF(i)
#define ST_FLUSH(wsp)

__device__ __forceinline__ unsigned lds_arrive(LAS unsigned* c) {
  unsigned o = 0;
  if ((threadIdx.x & 63) == 0)
    asm volatile("ds_add_rtn_u32 %0, %1, %2\n\ts_waitcnt lgkmcnt(0)" : "=&v"(o) : "v"((unsigned)(size_t)c), "v"(1u) : "memory");
  return __builtin_amdgcn_readfirstlane(o);
}
__device__ __forceinline__ void lds_wait(LAS unsigned* c, unsigned want) {
  for (unsigned spins = 0; spins < (1u << 22); ++spins) {
    unsigned v;
    asm volatile("ds_read_b32 %0, %1\n\ts_waitcnt lgkmcnt(0)" : "=&v"(v) : "v"((unsigned)(size_t)c) : "memory");
    if (__builtin_amdgcn_readfirstlane(v) >= want) break;
    __builtin_amdgcn_s_sleep(1);
  }
}
struct Tr14 { v2u h[14]; };
template <int OFF> __device__ __forceinline__ void tr14(Tr14& t, const unsigned (&ad)[7]) {
  asm volatile("ds_read_b64_tr_b16 %0, %14 offset:%21\n\tds_read_b64_tr_b16 %1, %14 offset:%22\n\t"
               "ds_read_b64_tr_b16 %2, %15 offset:%21\n\tds_read_b64_tr_b16 %3, %15 offset:%22\n\t"
               "ds_read_b64_tr_b16 %4, %16 offset:%21\n\tds_read_b64_tr_b16 %5, %16 offset:%22\n\t"
               "ds_read_b64_tr_b16 %6, %17 offset:%21\n\tds_read_b64_tr_b16 %7, %17 offset:%22\n\t"
               "ds_read_b64_tr_b16 %8, %18 offset:%21\n\tds_read_b64_tr_b16 %9, %18 offset:%22\n\t"
               "ds_read_b64_tr_b16 %10, %19 offset:%21\n\tds_read_b64_tr_b16 %11, %19 offset:%22\n\t"
               "ds_read_b64_tr_b16 %12, %20 offset:%21\n\tds_read_b64_tr_b16 %13, %20 offset:%22"
               : "=&v"(t.h[0]), "=&v"(t.h[1]), "=&v"(t.h[2]), "=&v"(t.h[3]), "=&v"(t.h[4]), "=&v"(t.h[5]), "=&v"(t.h[6]),
                 "=&v"(t.h[7]), "=&v"(t.h[8]), "=&v"(t.h[9]), "=&v"(t.h[10]), "=&v"(t.h[11]), "=&v"(t.h[12]), "=&v"(t.h[13])
               : "v"(ad[0]), "v"(ad[1]), "v"(ad[2]), "v"(ad[3]), "v"(ad[4]), "v"(ad[5]), "v"(ad[6]), "n"(OFF), "n"(OFF + 1024)
               : "memory");
}
template <int OFF> __device__ __forceinline__ void rd2x(v4u (&x)[2], unsigned ad) {
  asm volatile("ds_read_b128 %0, %2 offset:%3\n\tds_read_b128 %1, %2 offset:%4" : "=&v"(x[0]), "=&v"(x[1]) : "v"(ad), "n"(OFF), "n"(OFF + 2048) : "memory");
}
template <int N> __device__ __forceinline__ void waitl_pin(Tr14& t, v4u (&x)[2]) {
  asm volatile("s_waitcnt lgkmcnt(%16)"
               : "+v"(t.h[0]), "+v"(t.h[1]), "+v"(t.h[2]), "+v"(t.h[3]), "+v"(t.h[4]), "+v"(t.h[5]), "+v"(t.h[6]), "+v"(t.h[7]), "+v"(t.h[8]),
                 "+v"(t.h[9]), "+v"(t.h[10]), "+v"(t.h[11]), "+v"(t.h[12]), "+v"(t.h[13]), "+v"(x[0]), "+v"(x[1])
               : "n"(N) : "memory");
}
template <int OFF> __device__ __forceinline__ void tr2(v2u& h0, v2u& h1, unsigned ad) {
  asm volatile("ds_read_b64_tr_b16 %0, %2 offset:%3\n\tds_read_b64_tr_b16 %1, %2 offset:%4" : "=&v"(h0), "=&v"(h1) : "v"(ad), "n"(OFF), "n"(OFF + 1024) : "memory");
}
__device__ __forceinline__ void keep(const Tr14& t, const v4u (&x)[2]) {
  asm volatile("" :: "v"(t.h[0]), "v"(t.h[1]), "v"(t.h[2]), "v"(t.h[3]), "v"(t.h[4]), "v"(t.h[5]), "v"(t.h[6]), "v"(t.h[7]), "v"(t.h[8]),
               "v"(t.h[9]), "v"(t.h[10]), "v"(t.h[11]), "v"(t.h[12]), "v"(t.h[13]), "v"(x[0]), "v"(x[1]));
}
__device__ __forceinline__ void waitl_pinS(v4u (&x)[2], v2u (&t)[4]) {
  asm volatile("s_waitcnt lgkmcnt(0)" : "+v"(x[0]), "+v"(x[1]), "+v"(t[0]), "+v"(t[1]), "+v"(t[2]), "+v"(t[3]) :: "memory");
}
template <int OFF> __device__ __forceinline__ void tr4(v2u (&h)[4], unsigned a0, unsigned a1) {
  asm volatile("ds_read_b64_tr_b16 %0, %4 offset:%6\n\tds_read_b64_tr_b16 %1, %4 offset:%7\n\t"
               "ds_read_b64_tr_b16 %2, %5 offset:%6\n\tds_read_b64_tr_b16 %3, %5 offset:%7"
               : "=&v"(h[0]), "=&v"(h[1]), "=&v"(h[2]), "=&v"(h[3]) : "v"(a0), "v"(a1), "n"(OFF), "n"(OFF + 1024) : "memory");
}
template <int OFF> __device__ __forceinline__ void rd4w(v4u (&x)[4], unsigned ad) {
  asm volatile("ds_read_b128 %0, %4 offset:%5\n\tds_read_b128 %1, %4 offset:%6\n\tds_read_b128 %2, %4 offset:%7\n\tds_read_b128 %3, %4 offset:%8"
               : "=&v"(x[0]), "=&v"(x[1]), "=&v"(x[2]), "=&v"(x[3]) : "v"(ad), "n"(OFF), "n"(OFF + 2048), "n"(OFF + 4096), "n"(OFF + 6144) : "memory");
}
template <int OFF> __device__ __forceinline__ void rd1(v4u& x, unsigned ad) {
  asm volatile("ds_read_b128 %0, %1 offset:%2" : "=&v"(x) : "v"(ad), "n"(OFF) : "memory");
}
template <int N> __device__ __forceinline__ void waitl_pin5(v4u (&x)[4], v4u& y) {
  asm volatile("s_waitcnt lgkmcnt(%5)" : "+v"(x[0]), "+v"(x[1]), "+v"(x[2]), "+v"(x[3]), "+v"(y) : "n"(N) : "memory");
}
template <int N> __device__ __forceinline__ void waitl_pin4(v4u (&x)[4]) {
  asm volatile("s_waitcnt lgkmcnt(%4)" : "+v"(x[0]), "+v"(x[1]), "+v"(x[2]), "+v"(x[3]) : "n"(N) : "memory");
}
__device__ __forceinline__ float fdiv(float a, float b) {
  const float rc = __builtin_amdgcn_rcpf(b), q0 = a * rc;
  return __builtin_fmaf(__builtin_fmaf(-q0, b, a), rc, q0);
}
typedef std::integral_constant<int, 0> I0;
#define IC(n) std::integral_constant<int, (n)>{}
#define CAS __attribute__((address_space(4)))
__device__ __forceinline__ void poll_flags(const char* p, int n, u64 tag) {
  const int lane = threadIdx.x & 63;
  GAS u64* f = (GAS u64*)p + (lane < n ? lane : 0);
  for (unsigned spins = 0; spins < SPIN_CAP; ++spins) {
    if (__all(__hip_atomic_load(f, RLX_AGENT) == tag)) break;
    __builtin_amdgcn_s_sleep(PSLEEP);
  }
  __builtin_amdgcn_fence(__ATOMIC_ACQUIRE, "wavefront");
}
__device__ __forceinline__ void aload4(v4u (&r)[4], unsigned voff, const char* sbase) {
  asm volatile("global_load_dwordx4 %0, %4, %5 sc1\n\tglobal_load_dwordx4 %1, %4, %5 offset:1024 sc1\n\t"
               "global_load_dwordx4 %2, %4, %5 offset:2048 sc1\n\tglobal_load_dwordx4 %3, %4, %5 offset:3072 sc1"
               : "=&v"(r[0]), "=&v"(r[1]), "=&v"(r[2]), "=&v"(r[3]) : "v"(voff), "s"(sbase) : "memory");
}
__device__ __forceinline__ void trread8(v4u (&wf)[4], unsigned a0, unsigned a1, unsigned a2, unsigned a3) {
  v2u x0, x1, x2, x3, x4, x5, x6, x7;
  asm volatile("ds_read_b64_tr_b16 %0, %8\n\tds_read_b64_tr_b16 %1, %8 offset:2048\n\t"
               "ds_read_b64_tr_b16 %2, %9\n\tds_read_b64_tr_b16 %3, %9 offset:2048\n\t"
               "ds_read_b64_tr_b16 %4, %10\n\tds_read_b64_tr_b16 %5, %10 offset:2048\n\t"
               "ds_read_b64_tr_b16 %6, %11\n\tds_read_b64_tr_b16 %7, %11 offset:2048\n\ts_waitcnt lgkmcnt(0)"
               : "=&v"(x0), "=&v"(x1), "=&v"(x2), "=&v"(x3), "=&v"(x4), "=&v"(x5), "=&v"(x6), "=&v"(x7)
               : "v"(a0), "v"(a1), "v"(a2), "v"(a3) : "memory");
  wf[0] = v4u{x0.x, x0.y, x1.x, x1.y}; wf[1] = v4u{x2.x, x2.y, x3.x, x3.y};
  wf[2] = v4u{x4.x, x4.y, x5.x, x5.y}; wf[3] = v4u{x6.x, x6.y, x7.x, x7.y};
}
constexpr int p2_wait(int i, int N) {
  int total = 0, need = 0;
  for (int j = 0; j < 4 && j < N; j++) { total += 4; if (j == i) need = total; }
  for (int s = 0; s < i; s++) {
    if (s + 3 < N) { total += 4; if (s + 3 == i) need = total; }
    if (s + 4 < N) { total += 4; if (s + 4 == i && need < total) need = total; }
  }
  return total - need;
}

template <int... I, class Fn> __device__ __forceinline__ void static_for_(std::integer_sequence<int, I...>, Fn&& f) { (f(std::integral_constant<int, I>{}), ...); }
template <int N, class Fn> __device__ __forceinline__ void static_for(Fn&& f) { static_for_(std::make_integer_sequence<int, N>{}, f); }

struct Args {
  const u16 *x, *Lre, *Lim, *Bre, *Bim, *Cre, *Cim, *logdt, *Dsk, *w1, *w2;
  char* ws; float* out;
};

template <bool FFN>
__device__ __forceinline__ void body(const Args& a, LAS char* L, const int w) {
  const int b = blockIdx.x, xcd = b & 7, idx = b >> 3;
  const int r = (xcd & 1) * 8 + (idx & 7), c = (xcd >> 1) * 4 + (idx >> 3);
  const int rt = 4 * xcd + (idx & 3), ct = idx >> 2;
  const int tid = threadIdx.x, lane = tid & 63, tok = lane & 15, g = lane >> 4, tg = w & 3;
  const int codd = c & 1;
  char* const ws = a.ws;
  LAS unsigned* CNT = (LAS unsigned*)(L + M_CNT);
  ST_DECL ST(0);
  const __amdgpu_buffer_rsrc_t rsw = __builtin_amdgcn_make_buffer_rsrc((void*)ws, 0, (int)WS_END, 0x00020000);
  const int r8 = lane >> 3, p8 = lane & 7;
  constexpr int N2 = FFN ? 14 : 8;
  const int s0 = FFN ? 14 * tg : 56 + 8 * tg;
  const char* bw0 = (const char*)(a.w2 + 64 * ct);
  const unsigned bwl = (r8 * D_ + (((((p8 >> 1) ^ ((r8 >> 1) & 3)) << 1) | (p8 & 1)) << 3)) * 2;
  const int r4 = lane >> 2, c4 = lane & 3;
  const size_t bc0 = (size_t)(64 * ct) * 1024;
  const unsigned bcl = (r4 * 512 + ((c4 ^ ((-(r4 >> 2)) & 3)) << 3)) * 2;
  auto ringp = [&](int i) { const int e = i % 3; return L + (FFN ? 65536 + tg * 12288 + e * 4096 : (e == 0 ? 114688 + tg * 4096 : 32768 + tg * 8192 + (e - 1) * 4096)); };
  auto srcF = [&](int f, int i, int j) { return bw0 + (size_t)(32 * (14 * f + i) + 8 * j) * (D_ * 2) + bwl; };
  auto srcS = [&](int f, int i, int j) { return (const char*)((i & 1) ? a.Cim : a.Cre) + bc0 + 256 * f + 64 * (i >> 1) + (size_t)j * 16 * 1024 + bcl; };
  auto issueB = [&](int i) {
    LAS char* sl = ringp(i);
#pragma unroll
    for (int j = 0; j < 4; j++) glds16(FFN ? srcF(tg, i, j) : srcS(tg, i, j), sl + j * 1024);
  };
  const int swz = (tok >> 1) & 7;
  const int xoff = (32 * tg + tok) * 128 + ((g ^ swz) << 4);
  const int q = tok >> 2, p4 = lane & 3, fl = q | ((g & 1) << 2);
  const unsigned lb = (unsigned)(size_t)L;
  const unsigned xad[4] = {lb + xoff, lb + (xoff ^ 64), lb + xoff + 2 * SLOT, lb + (xoff ^ 64) + 2 * SLOT};
  auto waddr = [&](int v) { const int pi = codd ? (v == 6 ? 1 : v + 2) : v; return lb + OFF_W + (8 * g + q) * 256 + ((pi ^ fl) << 5) + p4 * 8; };
  constexpr unsigned ONES = 0x3f803f80u;
  LAS f32x2* STT = (LAS f32x2*)(L + M_STT);
  LAS float* CS = (LAS float*)(L + M_CS);
  u64 tag;

  if (FFN) {
    tag = *(const CAS u64*)(ws + WS_EP) + 1ull;
    if (tid < 16) CNT[tid] = 0u;
    if (w == 0 && lane < 32) {
      const float lr = bf2f(a.Lre[32 * c + lane]), li = bf2f(a.Lim[32 * c + lane]), dt = rbf(expf(bf2f(a.logdt[32 * c + lane])));
      const float er = expf(dt * lr); float sn, cn; sincosf(dt * li, &sn, &cn);
      const float br = er * cn, bi = er * sn, den = 1.f / (lr * lr + li * li), nr = br - 1.f;
      *(LAS f32x4*)(L + M_PRM + lane * 16) = f32x4{br, bi, (nr * lr + bi * li) * den, (bi * lr - nr * li) * den};
    }
    unsigned wlo[7], whi[7];
#pragma unroll
    for (int v = 0; v < 7; v++) { wlo[v] = waddr(v); whi[v] = wlo[v] + 2 * SLOT; }
    f32x4 acc[7][2];
#pragma unroll
    for (int u = 0; u < 7; u++) { acc[u][0] = f32x4{0, 0, 0, 0}; acc[u][1] = f32x4{0, 0, 0, 0}; }
    v4u xf[2][2]; Tr14 wt[2];
    auto rd = [&](auto kc) {
      constexpr int ks = decltype(kc)::value, kk = ks & 1, sl = (ks >> 1) & 3;
      rd2x<(sl & 1) * SLOT>(xf[kk], xad[(sl >> 1) * 2 + kk]);
      tr14<(sl & 1) * SLOT + kk * 8192>(wt[kk], (sl >> 1) ? whi : wlo);
    };
    auto step = [&](auto kc) {
      constexpr int ks = decltype(kc)::value, kk = ks & 1, kn = ks + 1, nk = kn & 1, sl = (kn >> 1) & 3;
      static_for<14>([&](auto mc) {
        constexpr int m = decltype(mc)::value, v = m >> 1, tt = m & 1;
        const v4u wv = {wt[kk].h[2 * v].x, wt[kk].h[2 * v].y, wt[kk].h[2 * v + 1].x, wt[kk].h[2 * v + 1].y};
        acc[v][tt] = mfma(wv, xf[kk][tt], acc[v][tt]);
        SB0();
        if constexpr (kn < 16) {
          if (m == 0) rd2x<(sl & 1) * SLOT>(xf[nk], xad[(sl >> 1) * 2 + nk]);
          if constexpr (m >= 1 && m <= 7) tr2<(sl & 1) * SLOT + nk * 8192>(wt[nk].h[2 * (m - 1)], wt[nk].h[2 * (m - 1) + 1], (sl >> 1) ? whi[m - 1] : wlo[m - 1]);
          SB0();
        }
      });
      keep(wt[kk], xf[kk]);
      if constexpr (kn < 16) waitl_pin<0>(wt[nk], xf[nk]);
    };
    WAITL0(); BAR();
    ST(1);
    rd(IC(0));
    waitl_pin<0>(wt[0], xf[0]);
    static_for<8>([&](auto sc) {
      constexpr int s = decltype(sc)::value;
      SF(1);
      step(IC(2 * s));
      SF(5);
      BAR();
      SF(6);
      if (s == 3) ST(2);
      step(IC(2 * s + 1));
      SF(8);
    });
    ST(3);
    BAR();
    ST(4);
    {
      __builtin_amdgcn_s_setprio(3);
      const f32x2 st0 = STT[32 * tg + tok], st1 = STT[32 * tg + 16 + tok];
      f32x4 cs[7];
#pragma unroll
      for (int v = 0; v < 7; v++) cs[v] = *(LAS f32x4*)(CS + v * 16 + 4 * g);
      const int T16 = 8 * r + 2 * tg;
      const int fb = (int)WS_H + ((T16 >> 2) * KS2 * 4 + (T16 & 3)) * 1024 + lane * 16;
      const float km[2] = {-st0.x * st0.y, -st1.x * st1.y}, rs[2] = {st0.y, st1.y};
      v2u o[2][2];
#pragma unroll
      for (int v = 0; v < 7; v++) {
#pragma unroll
        for (int tt = 0; tt < 2; tt++) {
          unsigned pk[2];
#pragma unroll
          for (int hh = 0; hh < 2; hh++) {
            const f32x2 ac = {acc[v][tt][2 * hh], acc[v][tt][2 * hh + 1]}, cc = {cs[v][2 * hh], cs[v][2 * hh + 1]};
            const f32x2 u = __builtin_elementwise_fma(ac, f32x2{rs[tt], rs[tt]}, cc * km[tt]);
            unsigned hb = pkbf(u.x, u.y);
            asm volatile("" : "+v"(hb));
            pk[hh] = silu2(hb);
          }
          o[v & 1][tt] = v2u{pk[0], pk[1]};
        }
        if (v & 1) {
          const int U = 7 * c + v - 1 + codd;
#pragma unroll
          for (int tt = 0; tt < 2; tt++)
            __builtin_amdgcn_raw_buffer_store_b128(v4u{o[0][tt].x, o[0][tt].y, o[1][tt].x, o[1][tt].y}, rsw, fb + tt * 1024 + (U >> 1) * 4096, 0, FAUX);
        } else if (v == 6) {
          const int U = codd ? 7 * c : 7 * c + 6;
#pragma unroll
          for (int tt = 0; tt < 2; tt++)
            __builtin_amdgcn_raw_buffer_store_b64(o[0][tt], rsw, fb + tt * 1024 + (U >> 1) * 4096 + (U & 1) * 8, 0, FAUX);
        }
      }
    }
    ST(5);
    lds_arrive(CNT + 7);
    waitv<0>();
    if (lds_arrive(CNT + 3) == 3u && lane == 0) __hip_atomic_store((GAS u64*)(ws + WS_FL + r * 256 + c * 8), tag, RLX_AGENT);
    ST(6);
    __builtin_amdgcn_s_setprio(0);
  } else {
    unsigned sx[2], sw[2];
#pragma unroll
    for (int h = 0; h < 2; h++) {
      sx[h] = ((8 * h + r8) * D_ + ((p8 ^ ((4 * h + (r8 >> 1)) & 7)) << 3)) * 2;
      sw[h] = ((8 * h + g) * F_ + (((((tok >> 1) ^ (g | (h << 2))) << 1) | (tok & 1)) << 3)) * 2;
    }
    const char* xb = (const char*)(a.x + (size_t)(128 * r + 32 * tg) * D_);
    const char* wb1 = (const char*)(a.w1 + (size_t)(16 * tg) * F_ + 112 * c - 16 * codd);
    const int rot = idx & 7;
    auto issue1 = [&](int s, int j) {
      LAS char* sb = L + (s & 3) * SLOT;
      const int k0 = ((s + rot) & 7) * 64;
      if (j < 4) glds16(xb + ((j >> 1) * 16 * D_ + k0) * 2 + sx[j & 1], sb + (4 * tg + j) * 1024);
      else glds16(wb1 + (size_t)(k0 + (j & 1) * 4) * F_ * 2 + sw[(j - 4) >> 1], sb + OFF_W + (4 * tg + j - 4) * 1024);
    };
    auto issue = [&](int s) {
#pragma unroll
      for (int j = 0; j < 8; j++) issue1(s, j);
    };
    issue(0); issue(1); issue(2); issue(3);
    __builtin_amdgcn_s_setprio(3);
    tag = *(const CAS u64*)(ws + WS_EP) + 1ull;
    const u16* ssp[2];
#pragma unroll
    for (int h = 0; h < 2; h++) {
      const int n = 16 * tg + 8 * h + r8;
      ssp[h] = ((n & 1) ? a.Bim : a.Bre) + (size_t)(32 * c + (n >> 1)) * D_ + ((p8 ^ ((n >> 1) & 7)) << 3);
    }
    auto issueS1 = [&](int js, int j) { glds16(ssp[j & 1] + ((4 * js + (j >> 1) + rot) & 7) * 64, L + js * SLOT + (j >> 1) * 8192 + (2 * tg + (j & 1)) * 1024); };
    auto issueR1 = [&](int i, int j) {
      const int o = 32 * i + 8 * tg + j;
      if (o < 48) glds16(srcF(o / 12, (o % 12) >> 2, o & 3), L + 65536 + o * 1024);
      else glds16(srcS((o - 48) >> 2, 0, o & 3), L + 65536 + o * 1024);
    };
    auto piece = [&](int s, int j) {
      if (s <= 3) issue1(s + 4, j); else if (s <= 5) issueS1(s - 4, j); else issueR1(s - 6, j);
    };
    const unsigned wa0 = waddr(tg), wa1 = waddr(tg == 3 ? 3 : tg + 4);
    const v4u ones = {ONES, ONES, ONES, ONES};
    v4u xr[16][2];
    v2u wq[2][4];
    f32x4 sm[2] = {{0, 0, 0, 0}, {0, 0, 0, 0}}, sq[2] = {{0, 0, 0, 0}, {0, 0, 0, 0}}, cf[2] = {{0, 0, 0, 0}, {0, 0, 0, 0}};
    auto rdS = [&](auto kc) {
      constexpr int ks = decltype(kc)::value, kk = ks & 1, sl = (ks >> 1) & 3;
      rd2x<(sl & 1) * SLOT>(xr[ks], xad[(sl >> 1) * 2 + kk]);
      tr4<(sl & 1) * SLOT + kk * 8192>(wq[kk], wa0 + (sl >> 1) * 2 * SLOT, wa1 + (sl >> 1) * 2 * SLOT);
    };
    auto pinS = [&](auto kc) {
      constexpr int ks = decltype(kc)::value, kk = ks & 1;
      waitl_pinS(xr[ks], wq[kk]);
    };
    auto stat = [&](auto kc, int s, int j0) {
      constexpr int ks = decltype(kc)::value, kk = ks & 1;
      sm[0] = mfma(ones, xr[ks][0], sm[0]); sq[0] = mfma(xr[ks][0], xr[ks][0], sq[0]);
      SB0(); if (j0 >= 0) piece(s, j0); SB0();
      sm[1] = mfma(ones, xr[ks][1], sm[1]);
      SB0(); if (j0 >= 0) piece(s, j0 + 1); SB0();
      sq[1] = mfma(xr[ks][1], xr[ks][1], sq[1]);
      SB0(); if (j0 >= 0) piece(s, j0 + 2); SB0();
      cf[0] = mfma(v4u{wq[kk][0].x, wq[kk][0].y, wq[kk][1].x, wq[kk][1].y}, ones, cf[0]);
      SB0(); if (j0 >= 0) piece(s, j0 + 3); SB0();
      cf[1] = mfma(v4u{wq[kk][2].x, wq[kk][2].y, wq[kk][3].x, wq[kk][3].y}, ones, cf[1]);
      SB0();
    };
    const int sch = lane & 7, seg = lane >> 3, chg = 32 * c + 8 * tg + sch;
    cplx lam, coef, l16, l32, l64, lseg = {1.f, 0.f}, wa = {0.f, 0.f}, wb = {0.f, 0.f};
    waitv<24>(); BAR();
    ST(1);
    rdS(IC(0));
    pinS(IC(0));
    static_for<8>([&](auto sc) {
      constexpr int s = decltype(sc)::value;
      SF(1);
      rdS(IC(2 * s + 1));
      stat(IC(2 * s), s - 1, s >= 1 ? 4 : -1);
      pinS(IC(2 * s + 1));
      SF(2);
      if (s <= 6) waitv<16>();
      SF(3);
      BAR();
      SF(6);
      if (s == 3) ST(2);
      if constexpr (s < 7) rdS(IC(2 * s + 2));
      stat(IC(2 * s + 1), s, 0);
      if constexpr (s < 7) pinS(IC(2 * s + 2));
      SF(8);
      if (s == 1) {
        const f32x4 pr = *(LAS f32x4*)(L + M_PRM + (8 * tg + sch) * 16);
        lam = {pr[0], pr[1]}; coef = {pr[2], pr[3]};
        const cplx l2 = cmul(lam, lam), l4 = cmul(l2, l2), l8 = cmul(l4, l4);
        l16 = cmul(l8, l8); l32 = cmul(l16, l16); l64 = cmul(l32, l32);
        const cplx l128 = cmul(l64, l64), l256 = cmul(l128, l128), l512 = cmul(l256, l256), l1024 = cmul(l512, l512);
        if (seg & 1) lseg = l16;
        if (seg & 2) lseg = cmul(lseg, l32);
        if (seg & 4) lseg = cmul(lseg, l64);
        const int nb = r - 1 - (seg + 8);
        cplx t = {1.f, 0.f};
        const int ne = nb >= 0 ? nb : (nb + 8 >= 0 ? nb + 8 : 0);
        if (ne & 1) t = l128;
        if (ne & 2) t = cmul(t, l256);
        if (ne & 4) t = cmul(t, l512);
        if (nb >= 0) { wb = t; wa = cmul(t, l1024); } else if (nb + 8 >= 0) { wa = t; }
        asm volatile("" : "+v"(wa.r), "+v"(wa.i), "+v"(wb.r), "+v"(wb.i), "+v"(lseg.r), "+v"(lseg.i));
      }
    });
    {
      const int e = tok & 3;
      const float d0 = e == 0 ? sq[0][0] : e == 1 ? sq[0][1] : e == 2 ? sq[0][2] : sq[0][3];
      const float d1 = e == 0 ? sq[1][0] : e == 1 ? sq[1][1] : e == 2 ? sq[1][2] : sq[1][3];
      const bool own = g == q;
#pragma unroll
      for (int tt = 0; tt < 2; tt++) {
        const float m = sm[tt][0] * (1.f / 512.f);
        const float var = fmaxf((tt ? d1 : d0) * (1.f / 512.f) - m * m, 0.f);
        const float mb = rbf(m), sd = rbf(__builtin_amdgcn_sqrtf(rbf(rbf(var) + 1e-5f)));
        if (own) {
          STT[32 * tg + 16 * tt + tok] = f32x2{mb, fdiv(1.f, sd)};
          if (c == 0)
            __builtin_amdgcn_raw_buffer_store_b64(v2u{__builtin_bit_cast(unsigned, mb), __builtin_bit_cast(unsigned, sd)}, rsw,
                                                  (int)WS_ST + (128 * r + 32 * tg + 16 * tt + tok) * 8, 0, AUX_SC1);
        }
      }
      if (tok == 0) {
        *(LAS f32x4*)(CS + tg * 16 + 4 * g) = cf[0];
        if (tg < 3) *(LAS f32x4*)(CS + (tg + 4) * 16 + 4 * g) = cf[1];
      }
    }
    ST(3);
    WAITL0(); BAR();
    ST(4);
#pragma unroll
    for (int j = 4; j < 8; j++) piece(7, j);
    __builtin_amdgcn_s_setprio(0);
    f32x4 acc[4][2];
    f32x4 cs4 = {0, 0, 0, 0};
#pragma unroll
    for (int u = 0; u < 4; u++) { acc[u][0] = f32x4{0, 0, 0, 0}; acc[u][1] = f32x4{0, 0, 0, 0}; }
    const unsigned sad[2] = {lb + tok * 128 + ((g ^ swz) << 4), lb + ((tok * 128 + ((g ^ swz) << 4)) ^ 64)};
    v4u wf[3][4], wo[3];
    const unsigned oad[2] = {sad[0] + tg * 2048, sad[1] + tg * 2048};
    waitv<0>();
    lds_arrive(CNT + 0); lds_wait(CNT + 0, 4u);
    rd4w<0>(wf[0], sad[0]); rd1<0>(wo[0], oad[0]);
    rd4w<0>(wf[1], sad[1]); rd1<0>(wo[1], oad[1]);
    lds_wait(CNT + 7, 4u);
    static_for<16>([&](auto kc) {
      constexpr int ks = decltype(kc)::value, kn = ks + 2;
      if constexpr (kn < 16) {
        rd4w<(kn >> 3) * SLOT + ((kn >> 1) & 3) * 8192>(wf[kn % 3], sad[kn & 1]);
        rd1<(kn >> 3) * SLOT + ((kn >> 1) & 3) * 8192>(wo[kn % 3], oad[kn & 1]);
        waitl_pin5<10>(wf[ks % 3], wo[ks % 3]);
      } else if constexpr (kn == 16) waitl_pin5<5>(wf[ks % 3], wo[ks % 3]);
      else waitl_pin5<0>(wf[ks % 3], wo[ks % 3]);
      SB0();
#pragma unroll
      for (int u = 0; u < 4; u++) { acc[u][0] = mfma(wf[ks % 3][u], xr[ks][0], acc[u][0]); acc[u][1] = mfma(wf[ks % 3][u], xr[ks][1], acc[u][1]); }
      cs4 = mfma(wo[ks % 3], ones, cs4);
      if (ks == 8) lds_arrive(CNT + 1);
      SB0();
    });
    ST(5);
    __builtin_amdgcn_s_setprio(3);
    lds_wait(CNT + 1, 4u);
#pragma unroll
    for (int u = 0; u < 4; u++)
#pragma unroll
      for (int tt = 0; tt < 2; tt++) {
        const int token = 32 * tg + 16 * tt + tok;
        *(LAS f32x4*)(L + token * 256 + (((4 * u + g) ^ (token & 7) ^ (((token >> 4) & 3) << 2)) << 4)) = acc[u][tt];
      }
    const float ca = lane & 1 ? cs4[2] : cs4[0], cb = lane & 1 ? cs4[3] : cs4[1];
    const f32x2 c2 = {__shfl(ca, (sch >> 1) * 16 + (sch & 1)), __shfl(cb, (sch >> 1) * 16 + (sch & 1))};
    lds_arrive(CNT + 2);
    lds_wait(CNT + 2, 4u);
    cplx bu[16], h = {0.f, 0.f}, init0 = {0.f, 0.f};
    {
      const int rbase = seg * 4096 + (sch & 1) * 8, rch = (4 * tg + (sch >> 1)) ^ ((seg & 3) << 2);
      f32x2 v[16]; f32x4 st[8];
#pragma unroll
      for (int t = 0; t < 16; t++) v[t] = *(LAS f32x2*)(L + rbase + t * 256 + ((rch ^ (t & 7)) << 4));
#pragma unroll
      for (int t = 0; t < 8; t++) st[t] = *(LAS f32x4*)(L + M_STT + (16 * seg + 2 * t) * 8);
#pragma unroll
      for (int t = 0; t < 16; t++) {
        const f32x2 uu = (v[t] - st[t >> 1][(t & 1) * 2] * c2) * st[t >> 1][(t & 1) * 2 + 1];
        bu[t] = {uu.x, uu.y};
        h = cfma(lam, h, bu[t]);
      }
      { cplx o = {__shfl_up(h.r, 8), __shfl_up(h.i, 8)}; if (seg >= 1) h = cfma(l16, o, h); }
      { cplx o = {__shfl_up(h.r, 16), __shfl_up(h.i, 16)}; if (seg >= 2) h = cfma(l32, o, h); }
      { cplx o = {__shfl_up(h.r, 32), __shfl_up(h.i, 32)}; if (seg >= 4) h = cfma(l64, o, h); }
      if (seg == 7 && r < 15) {
        const unsigned ur = __builtin_bit_cast(unsigned, h.r), ui = __builtin_bit_cast(unsigned, h.i);
        GAS u64* gp = (GAS u64*)(ws + WS_E + ((size_t)r * 512 + chg) * 16);
        __hip_atomic_store(gp, ((u64)((unsigned)tag + hashA(ur, ui)) << 32) | ur, RLX_AGENT);
        __hip_atomic_store(gp + 1, ((u64)((unsigned)(tag >> 32) + hashB(ur, ui)) << 32) | ui, RLX_AGENT);
      }
      { cplx o = {__shfl_up(h.r, 8), __shfl_up(h.i, 8)}; if (seg >= 1) init0 = o; }
    }
    ST(6);
    issueB(1); issueB(2);
    cplx carry = {0.f, 0.f};
    if (r > 0) {
      const bool needA = seg < r, needB = seg + 8 < r;
      GAS u64* ga = (GAS u64*)(ws + WS_E + ((size_t)seg * 512 + chg) * 16);
      GAS u64* gb = ga + (size_t)8 * 512 * 2;
      u64 a0 = 0, a1 = 0, b0 = 0, b1 = 0;
      {
        GAS u64* gw = (GAS u64*)(ws + WS_E + ((size_t)(lane < r ? lane : 0) * 512 + 32 * c + 8 * tg) * 16);
        for (unsigned spins = 0; spins < SPIN_CAP; ++spins) {
          const u64 w0 = __hip_atomic_load(gw, RLX_AGENT), w1 = __hip_atomic_load(gw + 1, RLX_AGENT);
          if (__all((unsigned)(w0 >> 32) == (unsigned)tag + hashA((unsigned)w0, (unsigned)w1))) break;
          __builtin_amdgcn_s_sleep(PSLEEP);
        }
      }
      for (unsigned spins = 0; spins < SPIN_CAP; ++spins) {
        bool ok = true;
        if (needA) { a0 = __hip_atomic_load(ga, RLX_AGENT); a1 = __hip_atomic_load(ga + 1, RLX_AGENT); }
        if (needB) { b0 = __hip_atomic_load(gb, RLX_AGENT); b1 = __hip_atomic_load(gb + 1, RLX_AGENT); }
        if (needA) ok = (unsigned)(a0 >> 32) == (unsigned)tag + hashA((unsigned)a0, (unsigned)a1) &&
                        (unsigned)(a1 >> 32) == (unsigned)(tag >> 32) + hashB((unsigned)a0, (unsigned)a1);
        if (needB) ok = ok && (unsigned)(b0 >> 32) == (unsigned)tag + hashA((unsigned)b0, (unsigned)b1) &&
                        (unsigned)(b1 >> 32) == (unsigned)(tag >> 32) + hashB((unsigned)b0, (unsigned)b1);
        if (__all(ok)) break;
        __builtin_amdgcn_s_sleep(PSLEEP);
      }
      const cplx ea = {__builtin_bit_cast(float, (unsigned)a0), __builtin_bit_cast(float, (unsigned)a1)};
      const cplx eb = {__builtin_bit_cast(float, (unsigned)b0), __builtin_bit_cast(float, (unsigned)b1)};
      cplx s1 = {0.f, 0.f};
      if (needA) s1 = cmul(wa, ea);
      if (needB) s1 = cfma(wb, eb, s1);
      s1.r += __shfl_xor(s1.r, 8); s1.i += __shfl_xor(s1.i, 8);
      s1.r += __shfl_xor(s1.r, 16); s1.i += __shfl_xor(s1.i, 16);
      s1.r += __shfl_xor(s1.r, 32); s1.i += __shfl_xor(s1.i, 32);
      carry = s1;
    }
    ST(7);
    h = cfma(lseg, carry, init0);
    LAS char* tr = L + M_TR + tg * 4224;
#pragma unroll
    for (int t = 0; t < 16; t++) {
      h = cfma(lam, h, bu[t]);
      const cplx o = cmul(coef, h);
      const unsigned pk = pkbf(o.r, -o.i);
      LAS u16* dst = (LAS u16*)(tr + seg * 528 + t * 32 + sch * 2);
      dst[0] = (u16)pk; dst[8] = (u16)(pk >> 16);
    }
    CBAR();
#pragma unroll
    for (int j = 0; j < 4; j++) {
      const int part = j & 1, token = (j >> 1) * 64 + lane;
      const v4u v = *(LAS v4u*)(tr + (token >> 4) * 528 + (token & 15) * 32 + part * 16);
      const int T16 = 8 * r + (token >> 4);
      __builtin_amdgcn_raw_buffer_store_b128(v, rsw, (int)WS_H + (((T16 >> 2) * KS2 + 56 + 2 * c + part) * 4 + (T16 & 3)) * 1024 + ((token & 15) + 16 * tg) * 16, 0, AUX_SC1);
    }
    waitv<0>();
    if (lds_arrive(CNT + 4) == 3u && lane == 0) __hip_atomic_store((GAS u64*)(ws + WS_FL + r * 256 + 128 + c * 8), tag, RLX_AGENT);
    __builtin_amdgcn_s_setprio(1);
  }
  ST(8);
  v2u ex[2], ed[2];
#pragma unroll
  for (int j = 0; j < 2; j++) {
    const int it = tid + 512 * j, token = 64 * rt + (it >> 4), d = 64 * ct + 4 * (it & 15);
    ex[j] = *(const v2u*)(a.x + (size_t)token * D_ + d);
    ed[j] = *(const v2u*)(a.Dsk + d);
  }
  if (FFN) {
    if (tg == 0) { poll_flags(ws + WS_FL + (rt >> 1) * 256, 16, tag); lds_arrive(CNT + 6); }
    else lds_wait(CNT + 6, 1u);
  }
  else poll_flags(ws + WS_FL + (rt >> 1) * 256 + 128 + tg * 32, 4, tag);
  ST(9);
  const char* ap = ws + WS_H + ((size_t)rt * KS2 + s0) * 4096;
  v2u s2 = {0u, 0u};
  if (!FFN && tg == 0) s2 = __builtin_amdgcn_raw_buffer_load_b64(rsw, (int)WS_ST + (64 * rt + lane) * 8, 0, AUX_SC1);
  v4u ra[4][4];
#pragma unroll
  for (int i = 0; i < 4; i++) aload4(ra[i], lane * 16, ap + (size_t)i * 4096);
  if (FFN) lds_wait(CNT + 0, 4u);
  const int fw = (2 * g + (q >> 1)) & 3;
  int toff[4];
#pragma unroll
  for (int j = 0; j < 4; j++) toff[j] = (4 * g + q) * 128 + ((j ^ fw) << 5) + p4 * 8;
  const int coff = tok * 64 + ((g ^ ((-(tok >> 2)) & 3)) << 4);
  f32x4 acc2[4][4];
#pragma unroll
  for (int i = 0; i < 4; i++)
#pragma unroll
    for (int j = 0; j < 4; j++) acc2[i][j] = f32x4{0, 0, 0, 0};
  static_for<N2>([&](auto ic) {
    constexpr int i = decltype(ic)::value;
    waitv<p2_wait(i, N2)>();
    asm volatile("" : "+v"(ra[i & 3][0]), "+v"(ra[i & 3][1]), "+v"(ra[i & 3][2]), "+v"(ra[i & 3][3]));
    if (i == 0) ST(10);
    if (i == 4) ST(11);
    LAS char* sl = ringp(i);
    v4u wf[4];
    if (FFN) {
      const unsigned sb = (unsigned)(size_t)sl;
      trread8(wf, sb + toff[0], sb + toff[1], sb + toff[2], sb + toff[3]);
    } else {
#pragma unroll
      for (int j = 0; j < 4; j++) wf[j] = *(LAS v4u*)(sl + coff + j * 1024);
    }
#pragma unroll
    for (int j = 0; j < 4; j++)
#pragma unroll
      for (int t = 0; t < 4; t++) acc2[j][t] = mfma(wf[j], ra[i & 3][t], acc2[j][t]);
    SB0();
    if (i + 3 < N2) issueB(i + 3);
    if (i + 4 < N2) aload4(ra[i & 3], lane * 16, ap + (size_t)(i + 4) * 4096);
    SB0();
  });
  ST(12);
  BAR();
#pragma unroll
  for (int t = 0; t < 4; t++)
#pragma unroll
    for (int j = 0; j < 4; j++)
      *(LAS f32x4*)(L + t * SLOT + w * 4096 + (j * 4 + g) * 256 + ((tok + 4 * j + g) & 15) * 16) = acc2[j][t];
  if (!FFN && tg == 0) *(LAS v2u*)(L + M_ST2 + lane * 8) = s2;
  if (!FFN && b == 255 && w == 7) {
    poll_flags(ws + WS_FL + 15 * 256 + 128, 16, tag);
    if (lane == 0) __hip_atomic_store((GAS u64*)(ws + WS_EP), tag, RLX_AGENT);
  }
  WAITL0(); BAR();
  ST(13);
#pragma unroll
  for (int j = 0; j < 2; j++) {
    const int it = tid + 512 * j, tl = it >> 4, dq = it & 15;
    LAS char* pb = L + (tl >> 4) * SLOT + dq * 256 + (((tl & 15) + dq) & 15) * 16;
    const f32x4 ff = (*(LAS f32x4*)(pb) + *(LAS f32x4*)(pb + 4096)) + (*(LAS f32x4*)(pb + 8192) + *(LAS f32x4*)(pb + 12288));
    const f32x4 sy = (*(LAS f32x4*)(pb + 16384) + *(LAS f32x4*)(pb + 20480)) + (*(LAS f32x4*)(pb + 24576) + *(LAS f32x4*)(pb + 28672));
    const f32x2 st = *(LAS f32x2*)(L + M_ST2 + tl * 8);
    f32x4 res;
#pragma unroll
    for (int e = 0; e < 4; e++) {
      const unsigned xw = e < 2 ? ex[j].x : ex[j].y, dw = e < 2 ? ed[j].x : ed[j].y;
      const float xv = (e & 1) ? bfhi(xw) : bf2f(xw), dv = (e & 1) ? bfhi(dw) : bf2f(dw);
      const float xn = rbf(fdiv(rbf(xv - st.x), st.y));
      const float s = 2.f * sy[e] + rbf(xn * dv);
      const float gg = rbf(silu_f(rbf(ff[e])));
      res[e] = xv + s * gg;
    }
    *(f32x4*)(a.out + (size_t)(64 * rt + tl) * D_ + 64 * ct + 4 * dq) = res;
  }
  ST(14);
  ST_FLUSH(ws);
}

__global__ __launch_bounds__(512, 2) void k_fused(Args a) {
  extern __shared__ __attribute__((aligned(16))) char smem[];
  LAS char* L = (LAS char*)smem;
  const int w = __builtin_amdgcn_readfirstlane(threadIdx.x >> 6);
  if (w < 4) body<true>(a, L, w);
  else body<false>(a, L, w);
}
}

extern "C" void kernel_launch(void* const* d_in, const int* in_sizes, int n_in, void* d_out, int out_size, void* d_ws, size_t ws_size,
                              hipStream_t stream) {
  Args a;
  a.x = (const u16*)d_in[0]; a.Lre = (const u16*)d_in[1]; a.Lim = (const u16*)d_in[2];
  a.Bre = (const u16*)d_in[3]; a.Bim = (const u16*)d_in[4]; a.Cre = (const u16*)d_in[5]; a.Cim = (const u16*)d_in[6];
  a.logdt = (const u16*)d_in[7]; a.Dsk = (const u16*)d_in[8]; a.w1 = (const u16*)d_in[10]; a.w2 = (const u16*)d_in[11];
  a.ws = (char*)d_ws; a.out = (float*)d_out;
  (void)hipFuncSetAttribute((const void*)k_fused, hipFuncAttributeMaxDynamicSharedMemorySize, LDS_BYTES);
  k_fused<<<256, 512, LDS_BYTES, stream>>>(a);
}
```

```cpp
#include <hip/hip_runtime.h>
#include <hip/hip_bf16.h>
#include <stdint.h>
#include <utility>

typedef unsigned short u16;
typedef unsigned long long u64;
typedef unsigned v4u __attribute__((ext_vector_type(4)));
typedef unsigned v2u __attribute__((ext_vector_type(2)));
typedef float f32x4 __attribute__((ext_vector_type(4)));
typedef float f32x2 __attribute__((ext_vector_type(2)));
typedef short bf16x8 __attribute__((ext_vector_type(8)));
typedef short v4s __attribute__((ext_vector_type(4)));
typedef __bf16 v2bf __attribute__((ext_vector_type(2)));
#define LAS __attribute__((address_space(3)))
#define GAS __attribute__((address_space(1)))
#define RLX_AGENT __ATOMIC_RELAXED, __HIP_MEMORY_SCOPE_AGENT
#define RLX_WG __ATOMIC_RELAXED, __HIP_MEMORY_SCOPE_WORKGROUP

namespace {
constexpr int D_ = 512, F_ = 1792;
constexpr int KS2 = 88;
constexpr size_t WS_H = 0;
constexpr size_t WS_H_BYTES = (size_t)32 * KS2 * 4096;
constexpr size_t WS_ST = WS_H + WS_H_BYTES;
constexpr size_t WS_E = WS_ST + 2048 * 8;
constexpr size_t WS_FL = WS_E + 16 * 512 * 16;
constexpr size_t WS_EP = WS_FL + 16 * 256;
constexpr size_t WS_END = WS_EP + 128;
constexpr int SLOT = 32768, OFF_W = 16384;
constexpr int M_STT = 131072, M_CS = M_STT + 1024, M_PRM = M_CS + 512, M_ST2 = M_PRM + 512, M_CNT = M_ST2 + 512, M_TR = M_CNT + 256;
constexpr int LDS_BYTES = 163840;
constexpr unsigned CODE_BYTES = 47104;
constexpr unsigned SPIN_CAP = 1u << 17;
#define FAUX 16
constexpr int AUX_SC1 = 16;
#define RDM 0
#define GLM 4
#define WSLEEP 4
#define W2AT 1
#define PSLEEP 6

__device__ __forceinline__ float bf2f(unsigned b) { return __builtin_bit_cast(float, b << 16); }
__device__ __forceinline__ float bfhi(unsigned b) { return __builtin_bit_cast(float, b & 0xffff0000u); }
__device__ __forceinline__ unsigned pkbf(float lo, float hi) {
  v2bf v; v.x = (__bf16)lo; v.y = (__bf16)hi; return __builtin_bit_cast(unsigned, v);
}
__device__ __forceinline__ float rbf(float f) { return bf2f(pkbf(f, 0.f)); }
__device__ __forceinline__ f32x4 mfma(v4u a, v4u b, f32x4 c) {
  return __builtin_amdgcn_mfma_f32_16x16x32_bf16(__builtin_bit_cast(bf16x8, a), __builtin_bit_cast(bf16x8, b), c, 0, 0, 0);
}
__device__ __forceinline__ float dot2(unsigned a, unsigned b, float c) {
  return __builtin_amdgcn_fdot2_f32_bf16(__builtin_bit_cast(v2bf, a), __builtin_bit_cast(v2bf, b), c, false);
}
__device__ __forceinline__ void glds16(const void* g, LAS char* l) {
  __builtin_amdgcn_global_load_lds((const GAS void*)g, (LAS void*)l, 16, 0, 0);
}
__device__ __forceinline__ v2u trread(LAS char* p) {
  return __builtin_bit_cast(v2u, __builtin_amdgcn_ds_read_tr16_b64_v4i16((LAS v4s*)p));
}
__device__ __forceinline__ unsigned silu2(unsigned hb) {
  const f32x2 h = {bf2f(hb), bfhi(hb)};
  const f32x2 t = h * -1.44269504088896f;
  const f32x2 d = f32x2{__builtin_amdgcn_exp2f(t.x), __builtin_amdgcn_exp2f(t.y)} + 1.f;
  const f32x2 y = h * f32x2{__builtin_amdgcn_rcpf(d.x), __builtin_amdgcn_rcpf(d.y)};
  return pkbf(y.x, y.y);
}
struct cplx { float r, i; };
__device__ __forceinline__ float fma_(float a, float b, float c) { float d; asm("v_fma_f32 %0, %1, %2, %3" : "=v"(d) : "v"(a), "v"(b), "v"(c)); return d; }
__device__ __forceinline__ float fnma_(float a, float b, float c) { float d; asm("v_fma_f32 %0, -%1, %2, %3" : "=v"(d) : "v"(a), "v"(b), "v"(c)); return d; }
__device__ __forceinline__ float mul_(float a, float b) { float d; asm("v_mul_f32 %0, %1, %2" : "=v"(d) : "v"(a), "v"(b)); return d; }
__device__ __forceinline__ float add_(float a, float b) { float d; asm("v_add_f32 %0, %1, %2" : "=v"(d) : "v"(a), "v"(b)); return d; }
__device__ __forceinline__ float mul_t(float a, float b) { float d; asm("s_nop 0\n\tv_mul_f32 %0, %1, %2" : "=v"(d) : "v"(a), "v"(b)); return d; }
__device__ __forceinline__ float add_t(float a, float b) { float d; asm("s_nop 0\n\tv_add_f32 %0, %1, %2" : "=v"(d) : "v"(a), "v"(b)); return d; }
__device__ __forceinline__ float silu_f(float h) { return mul_t(h, __builtin_amdgcn_rcpf(add_t(1.f, __builtin_amdgcn_exp2f(mul_(h, -1.44269504088896f))))); }
__device__ __forceinline__ cplx cmul(cplx a, cplx b) { return {fnma_(a.i, b.i, mul_(a.r, b.r)), fma_(a.r, b.i, mul_(a.i, b.r))}; }
__device__ __forceinline__ cplx cfma(cplx a, cplx b, cplx c) {
  return {fma_(a.r, b.r, fnma_(a.i, b.i, c.r)), fma_(a.r, b.i, fma_(a.i, b.r, c.i))};
}
__device__ __forceinline__ unsigned hashA(unsigned a, unsigned b) { unsigned h = a * 0x9E3779B1u + (b ^ (b >> 15)) * 0x85EBCA77u; return h ^ (h >> 13); }
__device__ __forceinline__ unsigned hashB(unsigned a, unsigned b) { unsigned h = b * 0xC2B2AE3Du + (a ^ (a >> 16)) * 0x27D4EB2Fu; return h ^ (h >> 15); }
template <int N> __device__ __forceinline__ void waitv() { asm volatile("s_waitcnt vmcnt(%0)" ::"n"(N) : "memory"); }
#define WAITL0() __builtin_amdgcn_s_waitcnt(0xC07F)
#define BAR() __builtin_amdgcn_s_barrier()
#define SB0() __builtin_amdgcn_sched_barrier(0)
#define CBAR() asm volatile("" ::: "memory")
#define ST_DECL
#define ST(i)
#define SF(i)
#define ST_FLUSH(wsp)

__device__ __forceinline__ unsigned lds_arrive(LAS unsigned* c) {
  unsigned o = 0;
  if ((threadIdx.x & 63) == 0)
    asm volatile("s_waitcnt lgkmcnt(0)\n\tds_add_rtn_u32 %0, %1, %2\n\ts_waitcnt lgkmcnt(0)" : "=&v"(o) : "v"((unsigned)(size_t)c), "v"(1u) : "memory");
  return __builtin_amdgcn_readfirstlane(o);
}
__device__ __forceinline__ void lds_wait(LAS unsigned* c, unsigned want) {
#pragma nounroll
  for (unsigned spins = 0; spins < (1u << 22); ++spins) {
    unsigned v;
    asm volatile("ds_read_b32 %0, %1\n\ts_waitcnt lgkmcnt(0)" : "=&v"(v) : "v"((unsigned)(size_t)c) : "memory");
    if (__builtin_amdgcn_readfirstlane(v) >= want) break;
    __builtin_amdgcn_s_sleep(1);
  }
}
struct Tr14 { v2u h[14]; };
template <int OFF> __device__ __forceinline__ void tr14(Tr14& t, const unsigned (&ad)[7]) {
  asm volatile("ds_read_b64_tr_b16 %0, %14 offset:%21\n\tds_read_b64_tr_b16 %1, %14 offset:%22\n\t"
               "ds_read_b64_tr_b16 %2, %15 offset:%21\n\tds_read_b64_tr_b16 %3, %15 offset:%22\n\t"
               "ds_read_b64_tr_b16 %4, %16 offset:%21\n\tds_read_b64_tr_b16 %5, %16 offset:%22\n\t"
               "ds_read_b64_tr_b16 %6, %17 offset:%21\n\tds_read_b64_tr_b16 %7, %17 offset:%22\n\t"
               "ds_read_b64_tr_b16 %8, %18 offset:%21\n\tds_read_b64_tr_b16 %9, %18 offset:%22\n\t"
               "ds_read_b64_tr_b16 %10, %19 offset:%21\n\tds_read_b64_tr_b16 %11, %19 offset:%22\n\t"
               "ds_read_b64_tr_b16 %12, %20 offset:%21\n\tds_read_b64_tr_b16 %13, %20 offset:%22"
               : "=&v"(t.h[0]), "=&v"(t.h[1]), "=&v"(t.h[2]), "=&v"(t.h[3]), "=&v"(t.h[4]), "=&v"(t.h[5]), "=&v"(t.h[6]),
                 "=&v"(t.h[7]), "=&v"(t.h[8]), "=&v"(t.h[9]), "=&v"(t.h[10]), "=&v"(t.h[11]), "=&v"(t.h[12]), "=&v"(t.h[13])
               : "v"(ad[0]), "v"(ad[1]), "v"(ad[2]), "v"(ad[3]), "v"(ad[4]), "v"(ad[5]), "v"(ad[6]), "n"(OFF), "n"(OFF + 1024)
               : "memory");
}
template <int OFF> __device__ __forceinline__ void rd2x(v4u (&x)[2], unsigned ad) {
  asm volatile("ds_read_b128 %0, %2 offset:%3\n\tds_read_b128 %1, %2 offset:%4" : "=&v"(x[0]), "=&v"(x[1]) : "v"(ad), "n"(OFF), "n"(OFF + 2048) : "memory");
}
template <int N> __device__ __forceinline__ void waitl_pin(Tr14& t, v4u (&x)[2]) {
  asm volatile("s_waitcnt lgkmcnt(%16)"
               : "+v"(t.h[0]), "+v"(t.h[1]), "+v"(t.h[2]), "+v"(t.h[3]), "+v"(t.h[4]), "+v"(t.h[5]), "+v"(t.h[6]), "+v"(t.h[7]), "+v"(t.h[8]),
                 "+v"(t.h[9]), "+v"(t.h[10]), "+v"(t.h[11]), "+v"(t.h[12]), "+v"(t.h[13]), "+v"(x[0]), "+v"(x[1])
               : "n"(N) : "memory");
}
template <int OFF> __device__ __forceinline__ void tr2(v2u& h0, v2u& h1, unsigned ad) {
  asm volatile("ds_read_b64_tr_b16 %0, %2 offset:%3\n\tds_read_b64_tr_b16 %1, %2 offset:%4" : "=&v"(h0), "=&v"(h1) : "v"(ad), "n"(OFF), "n"(OFF + 1024) : "memory");
}
__device__ __forceinline__ void keep(const Tr14& t, const v4u (&x)[2]) {
  asm volatile("" :: "v"(t.h[0]), "v"(t.h[1]), "v"(t.h[2]), "v"(t.h[3]), "v"(t.h[4]), "v"(t.h[5]), "v"(t.h[6]), "v"(t.h[7]), "v"(t.h[8]),
               "v"(t.h[9]), "v"(t.h[10]), "v"(t.h[11]), "v"(t.h[12]), "v"(t.h[13]), "v"(x[0]), "v"(x[1]));
}
__device__ __forceinline__ void waitl_pinS(v4u (&x)[2], v2u (&t)[4]) {
  asm volatile("s_waitcnt lgkmcnt(0)" : "+v"(x[0]), "+v"(x[1]), "+v"(t[0]), "+v"(t[1]), "+v"(t[2]), "+v"(t[3]) :: "memory");
}
template <int OFF> __device__ __forceinline__ void tr4(v2u (&h)[4], unsigned a0, unsigned a1) {
  asm volatile("ds_read_b64_tr_b16 %0, %4 offset:%6\n\tds_read_b64_tr_b16 %1, %4 offset:%7\n\t"
               "ds_read_b64_tr_b16 %2, %5 offset:%6\n\tds_read_b64_tr_b16 %3, %5 offset:%7"
               : "=&v"(h[0]), "=&v"(h[1]), "=&v"(h[2]), "=&v"(h[3]) : "v"(a0), "v"(a1), "n"(OFF), "n"(OFF + 1024) : "memory");
}
template <int OFF> __device__ __forceinline__ void rd4w(v4u (&x)[4], unsigned ad) {
  asm volatile("ds_read_b128 %0, %4 offset:%5\n\tds_read_b128 %1, %4 offset:%6\n\tds_read_b128 %2, %4 offset:%7\n\tds_read_b128 %3, %4 offset:%8"
               : "=&v"(x[0]), "=&v"(x[1]), "=&v"(x[2]), "=&v"(x[3]) : "v"(ad), "n"(OFF), "n"(OFF + 2048), "n"(OFF + 4096), "n"(OFF + 6144) : "memory");
}
template <int OFF> __device__ __forceinline__ void rd1(v4u& x, unsigned ad) {
  asm volatile("ds_read_b128 %0, %1 offset:%2" : "=&v"(x) : "v"(ad), "n"(OFF) : "memory");
}
template <int N> __device__ __forceinline__ void waitl_pin5(v4u (&x)[4], v4u& y) {
  asm volatile("s_waitcnt lgkmcnt(%5)" : "+v"(x[0]), "+v"(x[1]), "+v"(x[2]), "+v"(x[3]), "+v"(y) : "n"(N) : "memory");
}
template <int N> __device__ __forceinline__ void waitl_pin4(v4u (&x)[4]) {
  asm volatile("s_waitcnt lgkmcnt(%4)" : "+v"(x[0]), "+v"(x[1]), "+v"(x[2]), "+v"(x[3]) : "n"(N) : "memory");
}
__device__ __forceinline__ float fdiv(float a, float b) {
  const float rc = __builtin_amdgcn_rcpf(b), q0 = mul_t(a, rc);
  return fma_(fnma_(q0, b, a), rc, q0);
}
typedef std::integral_constant<int, 0> I0;
#define IC(n) std::integral_constant<int, (n)>{}
#define CAS __attribute__((address_space(4)))
__device__ __forceinline__ void wload(unsigned& d, const char* p) { asm volatile("global_load_dword %0, %1, off" : "=&v"(d) : "v"(p) : "memory"); }
struct W8 { v2u h[8]; };
__device__ __forceinline__ void tr2k(v2u& h0, v2u& h1, unsigned ad) {
  asm volatile("ds_read_b64_tr_b16 %0, %2\n\tds_read_b64_tr_b16 %1, %2 offset:2048" : "=&v"(h0), "=&v"(h1) : "v"(ad) : "memory");
}
__device__ __forceinline__ void rd2h(v2u& h0, v2u& h1, unsigned ad) {
  asm volatile("ds_read_b64 %0, %2\n\tds_read_b64 %1, %2 offset:8" : "=&v"(h0), "=&v"(h1) : "v"(ad) : "memory");
}
__device__ __forceinline__ void waitl_pin8(W8& t) {
  asm volatile("s_waitcnt lgkmcnt(0)" : "+v"(t.h[0]), "+v"(t.h[1]), "+v"(t.h[2]), "+v"(t.h[3]), "+v"(t.h[4]), "+v"(t.h[5]), "+v"(t.h[6]), "+v"(t.h[7]) :: "memory");
}
__device__ __forceinline__ void keep8(const W8& t) {
  asm volatile("" :: "v"(t.h[0]), "v"(t.h[1]), "v"(t.h[2]), "v"(t.h[3]), "v"(t.h[4]), "v"(t.h[5]), "v"(t.h[6]), "v"(t.h[7]));
}
__device__ __forceinline__ void pin4(v4u (&x)[4]) { asm volatile("" : "+v"(x[0]), "+v"(x[1]), "+v"(x[2]), "+v"(x[3])); }
template <int OFF> __device__ __forceinline__ void aload1(v4u& r, unsigned voff, const char* sbase) {
  asm volatile("global_load_dwordx4 %0, %1, %2 offset:%3 sc1" : "=&v"(r) : "v"(voff), "s"(sbase), "n"(OFF) : "memory");
}
__device__ __forceinline__ void poll_flags(const char* p, int n, u64 tag) {
  const int lane = threadIdx.x & 63;
  GAS u64* f = (GAS u64*)p + (lane < n ? lane : 0);
#pragma nounroll
  for (unsigned spins = 0; spins < SPIN_CAP; ++spins) {
    if (__all(__hip_atomic_load(f, RLX_AGENT) == tag)) break;
    __builtin_amdgcn_s_sleep(PSLEEP);
  }
  __builtin_amdgcn_fence(__ATOMIC_ACQUIRE, "wavefront");
}
__device__ __forceinline__ void aload4(v4u (&r)[4], unsigned voff, const char* sbase) {
  asm volatile("global_load_dwordx4 %0, %4, %5 sc1\n\tglobal_load_dwordx4 %1, %4, %5 offset:1024 sc1\n\t"
               "global_load_dwordx4 %2, %4, %5 offset:2048 sc1\n\tglobal_load_dwordx4 %3, %4, %5 offset:3072 sc1"
               : "=&v"(r[0]), "=&v"(r[1]), "=&v"(r[2]), "=&v"(r[3]) : "v"(voff), "s"(sbase) : "memory");
}
__device__ __forceinline__ void trread8(v4u (&wf)[4], unsigned a0, unsigned a1, unsigned a2, unsigned a3) {
  v2u x0, x1, x2, x3, x4, x5, x6, x7;
  asm volatile("ds_read_b64_tr_b16 %0, %8\n\tds_read_b64_tr_b16 %1, %8 offset:2048\n\t"
               "ds_read_b64_tr_b16 %2, %9\n\tds_read_b64_tr_b16 %3, %9 offset:2048\n\t"
               "ds_read_b64_tr_b16 %4, %10\n\tds_read_b64_tr_b16 %5, %10 offset:2048\n\t"
               "ds_read_b64_tr_b16 %6, %11\n\tds_read_b64_tr_b16 %7, %11 offset:2048\n\ts_waitcnt lgkmcnt(0)"
               : "=&v"(x0), "=&v"(x1), "=&v"(x2), "=&v"(x3), "=&v"(x4), "=&v"(x5), "=&v"(x6), "=&v"(x7)
               : "v"(a0), "v"(a1), "v"(a2), "v"(a3) : "memory");
  wf[0] = v4u{x0.x, x0.y, x1.x, x1.y}; wf[1] = v4u{x2.x, x2.y, x3.x, x3.y};
  wf[2] = v4u{x4.x, x4.y, x5.x, x5.y}; wf[3] = v4u{x6.x, x6.y, x7.x, x7.y};
}
constexpr int p2_wait(int i, int N, int W, int RD) {
  int total = 0, need = 0;
  for (int j = 0; j < W && j < N; j++) { total += 4; if (j == i) need = total; }
  for (int s = 0; s < i; s++) {
    if (s + RD < N) { total += 4; if (s + RD == i + 1) need = total; }
    if (s + W < N) { total += 4; if (s + W == i) need = total > need ? total : need; }
  }
  return total - need;
}

template <int... I, class Fn> __device__ __forceinline__ void static_for_(std::integer_sequence<int, I...>, Fn&& f) { (f(std::integral_constant<int, I>{}), ...); }
template <int N, class Fn> __device__ __forceinline__ void static_for(Fn&& f) { static_for_(std::make_integer_sequence<int, N>{}, f); }

struct Args {
  const u16 *x, *Lre, *Lim, *Bre, *Bim, *Cre, *Cim, *logdt, *Dsk, *w1, *w2;
  char* ws; float* out;
};

template <bool FFN>
__device__ __forceinline__ void body(const Args& a, LAS char* L, const int w, v2u ctouch) {
  const int b = blockIdx.x, xcd = b & 7, idx = b >> 3;
  const int r = (xcd & 1) * 8 + (idx & 7), c = (xcd >> 1) * 4 + (idx >> 3);
  const int rt = 4 * xcd + (idx & 3), ct = idx >> 2;
  const int tid = threadIdx.x, lane = tid & 63, tok = lane & 15, g = lane >> 4, tg = w & 3;
  const int codd = c & 1;
  char* const ws = a.ws;
  LAS unsigned* CNT = (LAS unsigned*)(L + M_CNT);
  ST_DECL ST(0);
  const __amdgpu_buffer_rsrc_t rsw = __builtin_amdgcn_make_buffer_rsrc((void*)ws, 0, (int)WS_END, 0x00020000);
  const int r8 = lane >> 3, p8 = lane & 7;
  constexpr int N2 = FFN ? 14 : 8;
  const int s0 = FFN ? 14 * tg : 56 + 8 * tg;
  const char* bw0 = (const char*)(a.w2 + 64 * ct);
  const unsigned bwl = (r8 * D_ + (((((p8 >> 1) ^ ((r8 >> 1) & 3)) << 1) | (p8 & 1)) << 3)) * 2;
  const int r4 = lane >> 2, c4 = lane & 3;
  const size_t bc0 = (size_t)(64 * ct) * 1024;
  const unsigned bcl = (r4 * 512 + ((c4 ^ ((-(r4 >> 2)) & 3)) << 3)) * 2;
  constexpr int RD = FFN ? 3 : 5, WA = FFN ? 4 : 7;
  auto ringp = [&](int i) {
    const int e = i % RD;
    return L + (FFN ? 65536 + tg * 12288 + e * 4096 : e == 0 ? 114688 + tg * 4096 : e < 3 ? 32768 + tg * 8192 + (e - 1) * 4096 : tg * 8192 + (e - 3) * 4096);
  };
  auto srcF = [&](int f, int i, int j) { return bw0 + (size_t)(32 * (14 * f + i) + 8 * j) * (D_ * 2) + bwl; };
  auto srcS = [&](int f, int i, int j) { return (const char*)((i & 1) ? a.Cim : a.Cre) + bc0 + 256 * f + 64 * (i >> 1) + (size_t)j * 16 * 1024 + bcl; };
  auto issueB = [&](int i) {
    LAS char* sl = ringp(i);
#pragma unroll
    for (int j = 0; j < 4; j++) glds16(FFN ? srcF(tg, i, j) : srcS(tg, i, j), sl + j * 1024);
  };
  const int swz = (tok >> 1) & 7;
  const int xoff = (32 * tg + tok) * 128 + ((g ^ swz) << 4);
  const int q = tok >> 2, p4 = lane & 3, fl = q | ((g & 1) << 2);
  const unsigned lb = (unsigned)(size_t)L;
  const unsigned xad[4] = {lb + xoff, lb + (xoff ^ 64), lb + xoff + 2 * SLOT, lb + (xoff ^ 64) + 2 * SLOT};
  auto waddr = [&](int v) { const int pi = codd ? (v == 6 ? 1 : v + 2) : v; return lb + OFF_W + (8 * g + q) * 256 + ((pi ^ fl) << 5) + p4 * 8; };
  constexpr unsigned ONES = 0x3f803f80u;
  LAS f32x2* STT = (LAS f32x2*)(L + M_STT);
  LAS float* CS = (LAS float*)(L + M_CS);
  u64 tag;

  if (FFN) {
    tag = *(const CAS u64*)(ws + WS_EP) + 1ull;
    if (tid < 16) CNT[tid] = 0u;
    if (w == 0 && lane < 32) {
      const float lr = bf2f(a.Lre[32 * c + lane]), li = bf2f(a.Lim[32 * c + lane]), dt = rbf(expf(bf2f(a.logdt[32 * c + lane])));
      const float er = expf(dt * lr); float sn, cn; sincosf(dt * li, &sn, &cn);
      const float br = er * cn, bi = er * sn, den = 1.f / (lr * lr + li * li), nr = br - 1.f;
      *(LAS f32x4*)(L + M_PRM + lane * 16) = f32x4{br, bi, (nr * lr + bi * li) * den, (bi * lr - nr * li) * den};
    }
    unsigned warm_ = 0, warm2_ = 0;
    if (tid < 152) {
      const int t = b * 152 + tid;
      const char* p = t < 14336 ? (const char*)a.w1 + (size_t)t * 128
                    : t < 30720 ? (const char*)a.x + (size_t)(t - 14336) * 128
                    : t < 34816 ? (const char*)a.Bre + (size_t)(t - 30720) * 128 : (const char*)a.Bim + (size_t)(t - 34816) * 128;
      __builtin_amdgcn_s_sleep(WSLEEP);
      asm volatile("global_load_dword %0, %1, off" : "=&v"(warm_) : "v"(p) : "memory");
    }
    const int t2 = b * 88 + (tid < 88 ? tid : 0);
    const char* wp2 = t2 < 14336 ? (const char*)a.w2 + (size_t)t2 * 128
                    : t2 < 18432 ? (const char*)a.Cre + (size_t)(t2 - 14336) * 128 : (const char*)a.Cim + (size_t)(t2 - 18432) * 128;
    unsigned wlo[7], whi[7];
#pragma unroll
    for (int v = 0; v < 7; v++) { wlo[v] = waddr(v); whi[v] = wlo[v] + 2 * SLOT; }
    f32x4 acc[7][2];
#pragma unroll
    for (int u = 0; u < 7; u++) { acc[u][0] = f32x4{0, 0, 0, 0}; acc[u][1] = f32x4{0, 0, 0, 0}; }
    v4u xf[2][2]; Tr14 wt[2];
    auto rd = [&](auto kc) {
      constexpr int ks = decltype(kc)::value, kk = ks & 1, sl = (ks >> 1) & 3;
      rd2x<(sl & 1) * SLOT>(xf[kk], xad[(sl >> 1) * 2 + kk]);
      tr14<(sl & 1) * SLOT + kk * 8192>(wt[kk], (sl >> 1) ? whi : wlo);
    };
    auto step = [&](auto kc) {
      constexpr int ks = decltype(kc)::value, kk = ks & 1, kn = ks + 1, nk = kn & 1, sl = (kn >> 1) & 3;
      static_for<14>([&](auto mc) {
        constexpr int m = decltype(mc)::value, v = m >> 1, tt = m & 1;
        const v4u wv = {wt[kk].h[2 * v].x, wt[kk].h[2 * v].y, wt[kk].h[2 * v + 1].x, wt[kk].h[2 * v + 1].y};
        acc[v][tt] = mfma(wv, xf[kk][tt], acc[v][tt]);
        SB0();
        if constexpr (kn < 16) {
          if (m == 0) rd2x<(sl & 1) * SLOT>(xf[nk], xad[(sl >> 1) * 2 + nk]);
          if constexpr (m >= 1 && m <= 7) tr2<(sl & 1) * SLOT + nk * 8192>(wt[nk].h[2 * (m - 1)], wt[nk].h[2 * (m - 1) + 1], (sl >> 1) ? whi[m - 1] : wlo[m - 1]);
          SB0();
        }
      });
      keep(wt[kk], xf[kk]);
      if constexpr (kn < 16) waitl_pin<0>(wt[nk], xf[nk]);
    };
    WAITL0(); BAR();
    ST(1);
    rd(IC(0));
    waitl_pin<0>(wt[0], xf[0]);
    static_for<8>([&](auto sc) {
      constexpr int s = decltype(sc)::value;
      SF(1);
      step(IC(2 * s));
      SF(5);
      BAR();
      SF(6);
      if (s == 3) ST(2);
      if (s == W2AT && tg < 2) wload(warm2_, wp2);
      step(IC(2 * s + 1));
      SF(8);
    });
    asm volatile("s_waitcnt vmcnt(0)" : "+v"(warm_), "+v"(warm2_), "+v"(ctouch) :: "memory");
    ST(3);
    BAR();
    ST(4);
    {
      __builtin_amdgcn_s_setprio(3);
      const f32x2 st0 = STT[32 * tg + tok], st1 = STT[32 * tg + 16 + tok];
      f32x4 cs[7];
#pragma unroll
      for (int v = 0; v < 7; v++) cs[v] = *(LAS f32x4*)(CS + v * 16 + 4 * g);
      const int T16 = 8 * r + 2 * tg;
      const int fb = (int)WS_H + ((T16 >> 2) * KS2 * 4 + (T16 & 3)) * 1024 + lane * 16;
      const float km[2] = {-st0.x * st0.y, -st1.x * st1.y}, rs[2] = {st0.y, st1.y};
      v2u o[2][2];
#pragma unroll
      for (int v = 0; v < 7; v++) {
#pragma unroll
        for (int tt = 0; tt < 2; tt++) {
          unsigned pk[2];
#pragma unroll
          for (int hh = 0; hh < 2; hh++) {
            const f32x2 ac = {acc[v][tt][2 * hh], acc[v][tt][2 * hh + 1]}, cc = {cs[v][2 * hh], cs[v][2 * hh + 1]};
            const f32x2 u = __builtin_elementwise_fma(ac, f32x2{rs[tt], rs[tt]}, cc * km[tt]);
            unsigned hb = pkbf(u.x, u.y);
            asm volatile("" : "+v"(hb));
            pk[hh] = silu2(hb);
          }
          o[v & 1][tt] = v2u{pk[0], pk[1]};
        }
        if (v & 1) {
          const int U = 7 * c + v - 1 + codd;
#pragma unroll
          for (int tt = 0; tt < 2; tt++)
            __builtin_amdgcn_raw_buffer_store_b128(v4u{o[0][tt].x, o[0][tt].y, o[1][tt].x, o[1][tt].y}, rsw, fb + tt * 1024 + (U >> 1) * 4096, 0, FAUX);
        } else if (v == 6) {
          const int U = codd ? 7 * c : 7 * c + 6;
#pragma unroll
          for (int tt = 0; tt < 2; tt++)
            __builtin_amdgcn_raw_buffer_store_b64(o[0][tt], rsw, fb + tt * 1024 + (U >> 1) * 4096 + (U & 1) * 8, 0, FAUX);
        }
      }
    }
    ST(5);
    lds_arrive(CNT + 7);
    waitv<0>();
    if (lds_arrive(CNT + 3) == 3u) {
      lds_wait(CNT + 0, 4u);
      if (lane == 0) __hip_atomic_store((GAS u64*)(ws + WS_FL + r * 256 + c * 8), tag, RLX_AGENT);
    }
    ST(6);
    __builtin_amdgcn_s_setprio(0);
  } else {
    unsigned sx[2], sw[2];
#pragma unroll
    for (int h = 0; h < 2; h++) {
      sx[h] = ((8 * h + r8) * D_ + ((p8 ^ ((4 * h + (r8 >> 1)) & 7)) << 3)) * 2;
      sw[h] = ((8 * h + g) * F_ + (((((tok >> 1) ^ (g | (h << 2))) << 1) | (tok & 1)) << 3)) * 2;
    }
    const char* xb = (const char*)(a.x + (size_t)(128 * r + 32 * tg) * D_);
    const char* wb1 = (const char*)(a.w1 + (size_t)(16 * tg) * F_ + 112 * c - 16 * codd);
    const int rot = idx & 7;
    auto issue1 = [&](int s, int j) {
      LAS char* sb = L + (s & 3) * SLOT;
      const int k0 = ((s + rot) & 7) * 64;
      if (j < 4) glds16(xb + ((j >> 1) * 16 * D_ + k0) * 2 + sx[j & 1], sb + (4 * tg + j) * 1024);
      else glds16(wb1 + (size_t)(k0 + (j & 1) * 4) * F_ * 2 + sw[(j - 4) >> 1], sb + OFF_W + (4 * tg + j - 4) * 1024);
    };
    auto issue = [&](int s) {
#pragma unroll
      for (int j = 0; j < 8; j++) issue1(s, j);
    };
    issue(0); issue(1);
    __builtin_amdgcn_s_setprio(3);
    tag = *(const CAS u64*)(ws + WS_EP) + 1ull;
    const u16* ssp[2];
#pragma unroll
    for (int h = 0; h < 2; h++) {
      const int n = 16 * tg + 8 * h + r8;
      ssp[h] = ((n & 1) ? a.Bim : a.Bre) + (size_t)(32 * c + (n >> 1)) * D_ + ((p8 ^ ((n >> 1) & 7)) << 3);
    }
    auto issueS1 = [&](int js, int j) { glds16(ssp[j & 1] + ((4 * js + (j >> 1) + rot) & 7) * 64, L + js * SLOT + (j >> 1) * 8192 + (2 * tg + (j & 1)) * 1024); };
    auto issueR1 = [&](int i, int j) {
      const int o = 32 * i + 8 * tg + j;
      if (o < 48) glds16(srcF(o / 12, (o % 12) >> 2, o & 3), L + 65536 + o * 1024);
      else glds16(srcS((o - 48) >> 2, 0, o & 3), L + 65536 + o * 1024);
    };
    auto piece = [&](int s, int j) {
      if (s <= 3) issue1(s + 4, j); else if (s <= 5) issueS1(s - 4, j); else issueR1(s - 6, j);
    };
    const unsigned wa0 = waddr(tg), wa1 = waddr(tg == 3 ? 3 : tg + 4);
    const v4u ones = {ONES, ONES, ONES, ONES};
    v4u xr[16][2];
    v2u wq[2][4];
    f32x4 sm[2] = {{0, 0, 0, 0}, {0, 0, 0, 0}}, sq[2] = {{0, 0, 0, 0}, {0, 0, 0, 0}}, cf[2] = {{0, 0, 0, 0}, {0, 0, 0, 0}};
    auto rdS = [&](auto kc) {
      constexpr int ks = decltype(kc)::value, kk = ks & 1, sl = (ks >> 1) & 3;
      rd2x<(sl & 1) * SLOT>(xr[ks], xad[(sl >> 1) * 2 + kk]);
      tr4<(sl & 1) * SLOT + kk * 8192>(wq[kk], wa0 + (sl >> 1) * 2 * SLOT, wa1 + (sl >> 1) * 2 * SLOT);
    };
    auto pinS = [&](auto kc) {
      constexpr int ks = decltype(kc)::value, kk = ks & 1;
      waitl_pinS(xr[ks], wq[kk]);
    };
    auto stat = [&](auto kc, int s, int j0) {
      constexpr int ks = decltype(kc)::value, kk = ks & 1;
      sm[0] = mfma(ones, xr[ks][0], sm[0]); sq[0] = mfma(xr[ks][0], xr[ks][0], sq[0]);
      SB0(); if (j0 >= 0) piece(s, j0); SB0();
      sm[1] = mfma(ones, xr[ks][1], sm[1]);
      SB0(); if (j0 >= 0) piece(s, j0 + 1); SB0();
      sq[1] = mfma(xr[ks][1], xr[ks][1], sq[1]);
      SB0(); if (j0 >= 0) piece(s, j0 + 2); SB0();
      cf[0] = mfma(v4u{wq[kk][0].x, wq[kk][0].y, wq[kk][1].x, wq[kk][1].y}, ones, cf[0]);
      SB0(); if (j0 >= 0) piece(s, j0 + 3); SB0();
      cf[1] = mfma(v4u{wq[kk][2].x, wq[kk][2].y, wq[kk][3].x, wq[kk][3].y}, ones, cf[1]);
      SB0();
    };
    const int sch = lane & 7, seg = lane >> 3, chg = 32 * c + 8 * tg + sch;
    cplx lam, coef, l16, l32, l64, lseg = {1.f, 0.f}, wa = {0.f, 0.f}, wb = {0.f, 0.f};
    waitv<8>();
    BAR();
    ST(1);
    rdS(IC(0));
    issue(2);
    pinS(IC(0));
    static_for<8>([&](auto sc) {
      constexpr int s = decltype(sc)::value;
      SF(1);
      rdS(IC(2 * s + 1));
      if (s == 0) { stat(IC(0), -1, 0); issue1(3, 4); issue1(3, 5); issue1(3, 6); issue1(3, 7); }
      else stat(IC(2 * s), s - 1, 4);
      pinS(IC(2 * s + 1));
      SF(2);
      if (s <= 6) waitv<16>();
      SF(3);
      BAR();
      SF(6);
      if (s == 3) ST(2);
      if constexpr (s < 7) rdS(IC(2 * s + 2));
      stat(IC(2 * s + 1), s, 0);
      if constexpr (s < 7) pinS(IC(2 * s + 2));
      SF(8);
      if (s == 1) {
        const f32x4 pr = *(LAS f32x4*)(L + M_PRM + (8 * tg + sch) * 16);
        lam = {pr[0], pr[1]}; coef = {pr[2], pr[3]};
        const cplx l2 = cmul(lam, lam), l4 = cmul(l2, l2), l8 = cmul(l4, l4);
        l16 = cmul(l8, l8); l32 = cmul(l16, l16); l64 = cmul(l32, l32);
        const cplx l128 = cmul(l64, l64), l256 = cmul(l128, l128), l512 = cmul(l256, l256), l1024 = cmul(l512, l512);
        if (seg & 1) lseg = l16;
        if (seg & 2) lseg = cmul(lseg, l32);
        if (seg & 4) lseg = cmul(lseg, l64);
        const int nb = r - 1 - (seg + 8);
        cplx t = {1.f, 0.f};
        const int ne = nb >= 0 ? nb : (nb + 8 >= 0 ? nb + 8 : 0);
        if (ne & 1) t = l128;
        if (ne & 2) t = cmul(t, l256);
        if (ne & 4) t = cmul(t, l512);
        if (nb >= 0) { wb = t; wa = cmul(t, l1024); } else if (nb + 8 >= 0) { wa = t; }
        asm volatile("" : "+v"(wa.r), "+v"(wa.i), "+v"(wb.r), "+v"(wb.i), "+v"(lseg.r), "+v"(lseg.i));
      }
    });
    {
      const int e = tok & 3;
      const float d0 = e == 0 ? sq[0][0] : e == 1 ? sq[0][1] : e == 2 ? sq[0][2] : sq[0][3];
      const float d1 = e == 0 ? sq[1][0] : e == 1 ? sq[1][1] : e == 2 ? sq[1][2] : sq[1][3];
      const bool own = g == q;
#pragma unroll
      for (int tt = 0; tt < 2; tt++) {
        float m = sm[tt][0] * (1.f / 512.f);
        asm volatile("" : "+v"(m));
        const float var = fmaxf(fnma_(m, m, mul_(tt ? d1 : d0, 1.f / 512.f)), 0.f);
        const float mb = rbf(m), sd = rbf(__builtin_amdgcn_sqrtf(rbf(rbf(var) + 1e-5f)));
        if (own) {
          STT[32 * tg + 16 * tt + tok] = f32x2{mb, fdiv(1.f, sd)};
          if (c == 0)
            __builtin_amdgcn_raw_buffer_store_b64(v2u{__builtin_bit_cast(unsigned, mb), __builtin_bit_cast(unsigned, sd)}, rsw,
                                                  (int)WS_ST + (128 * r + 32 * tg + 16 * tt + tok) * 8, 0, AUX_SC1);
        }
      }
      if (tok == 0) {
        *(LAS f32x4*)(CS + tg * 16 + 4 * g) = cf[0];
        if (tg < 3) *(LAS f32x4*)(CS + (tg + 4) * 16 + 4 * g) = cf[1];
      }
    }
    ST(3);
    WAITL0(); BAR();
    ST(4);
#pragma unroll
    for (int j = 4; j < 8; j++) piece(7, j);
    __builtin_amdgcn_s_setprio(0);
    f32x4 acc[4][2];
    f32x4 cs4 = {0, 0, 0, 0};
#pragma unroll
    for (int u = 0; u < 4; u++) { acc[u][0] = f32x4{0, 0, 0, 0}; acc[u][1] = f32x4{0, 0, 0, 0}; }
    const unsigned sad[2] = {lb + tok * 128 + ((g ^ swz) << 4), lb + ((tok * 128 + ((g ^ swz) << 4)) ^ 64)};
    v4u wf[3][4], wo[3];
    const unsigned oad[2] = {sad[0] + tg * 2048, sad[1] + tg * 2048};
    waitv<0>();
    lds_arrive(CNT + 0); lds_wait(CNT + 0, 4u);
    rd4w<0>(wf[0], sad[0]); rd1<0>(wo[0], oad[0]);
    rd4w<0>(wf[1], sad[1]); rd1<0>(wo[1], oad[1]);
    lds_wait(CNT + 7, 4u);
    static_for<16>([&](auto kc) {
      constexpr int ks = decltype(kc)::value, kn = ks + 2;
      if constexpr (kn < 16) {
        rd4w<(kn >> 3) * SLOT + ((kn >> 1) & 3) * 8192>(wf[kn % 3], sad[kn & 1]);
        rd1<(kn >> 3) * SLOT + ((kn >> 1) & 3) * 8192>(wo[kn % 3], oad[kn & 1]);
        waitl_pin5<10>(wf[ks % 3], wo[ks % 3]);
      } else if constexpr (kn == 16) waitl_pin5<5>(wf[ks % 3], wo[ks % 3]);
      else waitl_pin5<0>(wf[ks % 3], wo[ks % 3]);
      SB0();
#pragma unroll
      for (int u = 0; u < 4; u++) { acc[u][0] = mfma(wf[ks % 3][u], xr[ks][0], acc[u][0]); acc[u][1] = mfma(wf[ks % 3][u], xr[ks][1], acc[u][1]); }
      cs4 = mfma(wo[ks % 3], ones, cs4);
      if (ks == 8) lds_arrive(CNT + 1);
      SB0();
    });
    ST(5);
    __builtin_amdgcn_s_setprio(3);
    lds_wait(CNT + 1, 4u);
#pragma unroll
    for (int u = 0; u < 4; u++)
#pragma unroll
      for (int tt = 0; tt < 2; tt++) {
        const int token = 32 * tg + 16 * tt + tok;
        *(LAS f32x4*)(L + token * 256 + (((4 * u + g) ^ (token & 7) ^ (((token >> 4) & 3) << 2)) << 4)) = acc[u][tt];
      }
    const float ca = lane & 1 ? cs4[2] : cs4[0], cb = lane & 1 ? cs4[3] : cs4[1];
    const f32x2 c2 = {__shfl(ca, (sch >> 1) * 16 + (sch & 1)), __shfl(cb, (sch >> 1) * 16 + (sch & 1))};
    lds_arrive(CNT + 2);
    lds_wait(CNT + 2, 4u);
    cplx bu[16], h = {0.f, 0.f}, init0 = {0.f, 0.f};
    asm volatile("" : "+v"(l16.r), "+v"(l16.i));
    l32 = cmul(l16, l16); l64 = cmul(l32, l32);
    {
      const int rbase = seg * 4096 + (sch & 1) * 8, rch = (4 * tg + (sch >> 1)) ^ ((seg & 3) << 2);
      f32x2 v[16]; f32x4 st[8];
#pragma unroll
      for (int t = 0; t < 16; t++) v[t] = *(LAS f32x2*)(L + rbase + t * 256 + ((rch ^ (t & 7)) << 4));
#pragma unroll
      for (int t = 0; t < 8; t++) st[t] = *(LAS f32x4*)(L + M_STT + (16 * seg + 2 * t) * 8);
#pragma unroll
      for (int t = 0; t < 16; t++) {
        const float mu = st[t >> 1][(t & 1) * 2], rs = st[t >> 1][(t & 1) * 2 + 1];
        bu[t] = {mul_(fnma_(mu, c2.x, v[t].x), rs), mul_(fnma_(mu, c2.y, v[t].y), rs)};
        h = cfma(lam, h, bu[t]);
      }
      { cplx o = {__shfl_up(h.r, 8), __shfl_up(h.i, 8)}; if (seg >= 1) h = cfma(l16, o, h); }
      { cplx o = {__shfl_up(h.r, 16), __shfl_up(h.i, 16)}; if (seg >= 2) h = cfma(l32, o, h); }
      { cplx o = {__shfl_up(h.r, 32), __shfl_up(h.i, 32)}; if (seg >= 4) h = cfma(l64, o, h); }
      if (seg == 7 && r < 15) {
        const unsigned ur = __builtin_bit_cast(unsigned, h.r), ui = __builtin_bit_cast(unsigned, h.i);
        GAS u64* gp = (GAS u64*)(ws + WS_E + ((size_t)r * 512 + chg) * 16);
        __hip_atomic_store(gp, ((u64)((unsigned)tag + hashA(ur, ui)) << 32) | ur, RLX_AGENT);
        __hip_atomic_store(gp + 1, ((u64)((unsigned)(tag >> 32) + hashB(ur, ui)) << 32) | ui, RLX_AGENT);
      }
      { cplx o = {__shfl_up(h.r, 8), __shfl_up(h.i, 8)}; if (seg >= 1) init0 = o; }
    }
    ST(6);
    lds_arrive(CNT + 5);
    issueB(1); issueB(2);
    cplx carry = {0.f, 0.f};
    if (r > 0) {
      const bool needA = seg < r, needB = seg + 8 < r;
      GAS u64* ga = (GAS u64*)(ws + WS_E + ((size_t)seg * 512 + chg) * 16);
      GAS u64* gb = ga + (size_t)8 * 512 * 2;
      u64 a0 = 0, a1 = 0, b0 = 0, b1 = 0;
#pragma nounroll
      for (unsigned spins = 0; spins < SPIN_CAP; ++spins) {
        bool ok = true;
        if (needA) { a0 = __hip_atomic_load(ga, RLX_AGENT); a1 = __hip_atomic_load(ga + 1, RLX_AGENT); }
        if (needB) { b0 = __hip_atomic_load(gb, RLX_AGENT); b1 = __hip_atomic_load(gb + 1, RLX_AGENT); }
        if (needA) ok = (unsigned)(a0 >> 32) == (unsigned)tag + hashA((unsigned)a0, (unsigned)a1) &&
                        (unsigned)(a1 >> 32) == (unsigned)(tag >> 32) + hashB((unsigned)a0, (unsigned)a1);
        if (needB) ok = ok && (unsigned)(b0 >> 32) == (unsigned)tag + hashA((unsigned)b0, (unsigned)b1) &&
                        (unsigned)(b1 >> 32) == (unsigned)(tag >> 32) + hashB((unsigned)b0, (unsigned)b1);
        if (__all(ok)) break;
        __builtin_amdgcn_s_sleep(2);
      }
      const cplx ea = {__builtin_bit_cast(float, (unsigned)a0), __builtin_bit_cast(float, (unsigned)a1)};
      const cplx eb = {__builtin_bit_cast(float, (unsigned)b0), __builtin_bit_cast(float, (unsigned)b1)};
      cplx s1 = {0.f, 0.f};
      if (needA) s1 = cmul(wa, ea);
      if (needB) s1 = cfma(wb, eb, s1);
      s1.r = add_(s1.r, __shfl_xor(s1.r, 8)); s1.i = add_(s1.i, __shfl_xor(s1.i, 8));
      s1.r = add_(s1.r, __shfl_xor(s1.r, 16)); s1.i = add_(s1.i, __shfl_xor(s1.i, 16));
      s1.r = add_(s1.r, __shfl_xor(s1.r, 32)); s1.i = add_(s1.i, __shfl_xor(s1.i, 32));
      carry = s1;
    }
    ST(7);
    h = cfma(lseg, carry, init0);
    LAS char* tr = L + M_TR + tg * 4224;
#pragma unroll
    for (int t = 0; t < 16; t++) {
      h = cfma(lam, h, bu[t]);
      const cplx o = cmul(coef, h);
      const unsigned pk = pkbf(o.r, -o.i);
      LAS u16* dst = (LAS u16*)(tr + seg * 528 + t * 32 + sch * 2);
      dst[0] = (u16)pk; dst[8] = (u16)(pk >> 16);
    }
    CBAR();
#pragma unroll
    for (int j = 0; j < 4; j++) {
      const int part = j & 1, token = (j >> 1) * 64 + lane;
      const v4u v = *(LAS v4u*)(tr + (token >> 4) * 528 + (token & 15) * 32 + part * 16);
      const int T16 = 8 * r + (token >> 4);
      __builtin_amdgcn_raw_buffer_store_b128(v, rsw, (int)WS_H + (((T16 >> 2) * KS2 + 56 + 2 * c + part) * 4 + (T16 & 3)) * 1024 + ((token & 15) + 16 * tg) * 16, 0, AUX_SC1);
    }
    waitv<0>();
    if (lds_arrive(CNT + 4) == 3u && lane == 0) __hip_atomic_store((GAS u64*)(ws + WS_FL + r * 256 + 128 + c * 8), tag, RLX_AGENT);
    __builtin_amdgcn_s_setprio(0);
    lds_wait(CNT + 5, 4u);
    issueB(3); issueB(4);
  }
  ST(8);
  v2u ex[4], ed; unsigned es[4][2];
  if (FFN) {
#pragma unroll
    for (int j = 0; j < 4; j++) ex[j] = *(const v2u*)(a.x + (size_t)(64 * rt + 16 * j + (tid >> 4)) * D_ + 64 * ct + 4 * (tid & 15));
    ed = *(const v2u*)(a.Dsk + 64 * ct + 4 * (tid & 15));
  }
  if (FFN) {
    if (tg == 0) { poll_flags(ws + WS_FL + (rt >> 1) * 256, 16, tag); lds_arrive(CNT + 6); }
    else lds_wait(CNT + 6, 1u);
  }
  else poll_flags(ws + WS_FL + (rt >> 1) * 256 + 128 + tg * 32, 4, tag);
  ST(9);
  const char* ap = ws + WS_H + ((size_t)rt * KS2 + s0) * 4096;
  if (FFN) {
#pragma unroll
    for (int j = 0; j < 4; j++) {
      const v2u t = __builtin_amdgcn_raw_buffer_load_b64(rsw, (int)WS_ST + (64 * rt + 16 * j + (tid >> 4)) * 8, 0, AUX_SC1);
      es[j][0] = t.x; es[j][1] = t.y;
    }
  }
  v4u ra[WA + 1][4];
#pragma unroll
  for (int i = 0; i < WA; i++) aload4(ra[i], lane * 16, ap + (size_t)i * 4096);
  if (FFN) lds_wait(CNT + 0, 4u);
  const int fw = (2 * g + (q >> 1)) & 3;
  unsigned toff[4];
#pragma unroll
  for (int j = 0; j < 4; j++) toff[j] = FFN ? (4 * g + q) * 128 + ((j ^ fw) << 5) + p4 * 8 : tok * 64 + ((g ^ ((-(tok >> 2)) & 3)) << 4) + j * 1024;
  f32x4 acc2[4][4];
#pragma unroll
  for (int i = 0; i < 4; i++)
#pragma unroll
    for (int j = 0; j < 4; j++) acc2[i][j] = f32x4{0, 0, 0, 0};
  W8 wf[2];
  auto rdw = [&](int i, int j) {
    const unsigned ad = (unsigned)(size_t)ringp(i) + toff[j];
    if (FFN) tr2k(wf[i & 1].h[2 * j], wf[i & 1].h[2 * j + 1], ad); else rd2h(wf[i & 1].h[2 * j], wf[i & 1].h[2 * j + 1], ad);
  };
#pragma unroll
  for (int j = 0; j < 4; j++) rdw(0, j);
  waitl_pin8(wf[0]);
  static_for<N2>([&](auto ic) {
    constexpr int i = decltype(ic)::value;
    waitv<p2_wait(i, N2, WA, RD)>();
    pin4(ra[i % (WA + 1)]);
    if (i == 0) ST(10);
    if (i == 4) ST(11);
    SB0();
    static_for<16>([&](auto mc) {
      constexpr int m = decltype(mc)::value, j = m >> 2, t = m & 3;
      const v4u wv = {wf[i & 1].h[2 * j].x, wf[i & 1].h[2 * j].y, wf[i & 1].h[2 * j + 1].x, wf[i & 1].h[2 * j + 1].y};
      acc2[j][t] = mfma(wv, ra[i % (WA + 1)][t], acc2[j][t]);
      SB0();
      if constexpr (m >= RDM && m < RDM + 4 && i + 1 < N2) rdw(i + 1, m - RDM);
      if constexpr (m >= GLM && m < GLM + 4 && i + RD < N2) glds16(FFN ? srcF(tg, i + RD, m - GLM) : srcS(tg, i + RD, m - GLM), ringp(i + RD) + (m - GLM) * 1024);
      if constexpr (m >= 8 && m < 12 && i + WA < N2) aload1<(m - 8) * 1024>(ra[(i + WA) % (WA + 1)][m - 8], lane * 16, ap + (size_t)(i + WA) * 4096);
      SB0();
    });
    keep8(wf[i & 1]);
    if constexpr (i + 1 < N2) waitl_pin8(wf[(i + 1) & 1]);
  });
  ST(12);
  auto pieceF = [&](int f, int t) { return L + (t < 3 ? 65536 + f * 12288 + t * 4096 : M_TR + f * 4096); };
  auto pieceS = [&](int f, int t) { return L + (t == 0 ? 114688 + f * 4096 : t < 3 ? 32768 + f * 8192 + (t - 1) * 4096 : f * 8192); };
  if (FFN) lds_wait(CNT + 4, 4u);
#pragma unroll
  for (int t = 0; t < 4; t++)
#pragma unroll
    for (int j = 0; j < 4; j++)
      *(LAS f32x4*)((FFN ? pieceF(tg, t) : pieceS(tg, t)) + (j * 4 + g) * 256 + ((tok + 4 * j + g) & 15) * 16) = acc2[j][t];
  lds_arrive(CNT + (FFN ? 8 : 9));
  if (!FFN) {
    if (b == 255 && w == 7) {
      poll_flags(ws + WS_FL + 15 * 256 + 128, 16, tag);
      if (lane == 0) __hip_atomic_store((GAS u64*)(ws + WS_EP), tag, RLX_AGENT);
    }
  } else {
    const int po = (tid & 15) * 256 + (((tid >> 4) + (tid & 15)) & 15) * 16;
    f32x4 sk[4], gg[4];
#pragma unroll
    for (int j = 0; j < 4; j++)
#pragma unroll
      for (int e = 0; e < 4; e++) {
        const unsigned xw = e < 2 ? ex[j].x : ex[j].y, dw = e < 2 ? ed.x : ed.y;
        const float xv = (e & 1) ? bfhi(xw) : bf2f(xw), dv = (e & 1) ? bfhi(dw) : bf2f(dw);
        sk[j][e] = rbf(mul_(rbf(fdiv(rbf(add_(xv, -__builtin_bit_cast(float, es[j][0]))), __builtin_bit_cast(float, es[j][1]))), dv));
      }
    lds_wait(CNT + 8, 4u);
#pragma unroll
    for (int j = 0; j < 4; j++) {
      const f32x4 p0 = *(LAS f32x4*)(pieceF(0, j) + po), p1 = *(LAS f32x4*)(pieceF(1, j) + po), p2 = *(LAS f32x4*)(pieceF(2, j) + po), p3 = *(LAS f32x4*)(pieceF(3, j) + po);
#pragma unroll
      for (int e = 0; e < 4; e++) gg[j][e] = rbf(silu_f(rbf(add_(add_(p0[e], p1[e]), add_(p2[e], p3[e])))));
    }
    lds_wait(CNT + 9, 4u);
    ST(13);
#pragma unroll
    for (int j = 0; j < 4; j++) {
      const f32x4 sy = (*(LAS f32x4*)(pieceS(0, j) + po) + *(LAS f32x4*)(pieceS(1, j) + po)) + (*(LAS f32x4*)(pieceS(2, j) + po) + *(LAS f32x4*)(pieceS(3, j) + po));
      const f32x4 xv = {bf2f(ex[j].x), bfhi(ex[j].x), bf2f(ex[j].y), bfhi(ex[j].y)};
      *(f32x4*)(a.out + (size_t)(64 * rt + 16 * j + (tid >> 4)) * D_ + 64 * ct + 4 * (tid & 15)) = xv + (2.f * sy + sk[j]) * gg[j];
    }
  }
  ST(14);
  ST_FLUSH(ws);
}

__global__ __launch_bounds__(512, 2) void k_fused(Args a) {
  extern __shared__ __attribute__((aligned(16))) char smem[];
  LAS char* L = (LAS char*)smem;
  const int w = __builtin_amdgcn_readfirstlane(threadIdx.x >> 6);
  v2u d0 = {0u, 0u};
  u64 pc; asm volatile("s_getpc_b64 %0" : "=s"(pc));
  if (w >= 1 && w < 4) {
    const unsigned o0 = (threadIdx.x - 64u) * 128u, o1 = o0 + 24576u < CODE_BYTES ? o0 + 24576u : 0u;
    asm volatile("global_load_dword %0, %2, %4\n\tglobal_load_dword %1, %3, %4" : "=&v"(d0.x), "=&v"(d0.y) : "v"(o0), "v"(o1), "s"(pc & ~127ull) : "memory");
  }
  if (w < 4) body<true>(a, L, w, d0);
  else body<false>(a, L, w, d0);
}
}

extern "C" void kernel_launch(void* const* d_in, const int* in_sizes, int n_in, void* d_out, int out_size, void* d_ws, size_t ws_size,
                              hipStream_t stream) {
  Args a;
  a.x = (const u16*)d_in[0]; a.Lre = (const u16*)d_in[1]; a.Lim = (const u16*)d_in[2];
  a.Bre = (const u16*)d_in[3]; a.Bim = (const u16*)d_in[4]; a.Cre = (const u16*)d_in[5]; a.Cim = (const u16*)d_in[6];
  a.logdt = (const u16*)d_in[7]; a.Dsk = (const u16*)d_in[8]; a.w1 = (const u16*)d_in[10]; a.w2 = (const u16*)d_in[11];
  a.ws = (char*)d_ws; a.out = (float*)d_out;
  (void)hipFuncSetAttribute((const void*)k_fused, hipFuncAttributeMaxDynamicSharedMemorySize, LDS_BYTES);
  k_fused<<<256, 512, LDS_BYTES, stream>>>(a);
}
```

```cpp
#include <hip/hip_runtime.h>
#include <hip/hip_bf16.h>
#include <stdint.h>
#include <utility>

typedef unsigned short u16;
typedef unsigned long long u64;
typedef unsigned v4u __attribute__((ext_vector_type(4)));
typedef unsigned v2u __attribute__((ext_vector_type(2)));
typedef float f32x4 __attribute__((ext_vector_type(4)));
typedef float f32x2 __attribute__((ext_vector_type(2)));
typedef short bf16x8 __attribute__((ext_vector_type(8)));
typedef short v4s __attribute__((ext_vector_type(4)));
typedef __bf16 v2bf __attribute__((ext_vector_type(2)));
#define LAS __attribute__((address_space(3)))
#define GAS __attribute__((address_space(1)))
#define RLX_AGENT __ATOMIC_RELAXED, __HIP_MEMORY_SCOPE_AGENT
#define RLX_WG __ATOMIC_RELAXED, __HIP_MEMORY_SCOPE_WORKGROUP

namespace {
constexpr int D_ = 512, F_ = 1792;
constexpr int KS2 = 88;
constexpr size_t WS_H = 0;
constexpr size_t WS_H_BYTES = (size_t)32 * KS2 * 4096;
constexpr size_t WS_ST = WS_H + WS_H_BYTES;
constexpr size_t WS_E = WS_ST + 2048 * 8;
constexpr size_t WS_FL = WS_E + 16 * 512 * 16;
constexpr size_t WS_EP = WS_FL + 16 * 256;
constexpr size_t WS_END = WS_EP + 128;
constexpr int SLOT = 32768, OFF_W = 16384;
constexpr int M_STT = 131072, M_CS = M_STT + 1024, M_PRM = M_CS + 512, M_ST2 = M_PRM + 512, M_CNT = M_ST2 + 512, M_TR = M_CNT + 256;
constexpr int LDS_BYTES = 163840;
constexpr unsigned CODE_BYTES = 47104;
constexpr unsigned SPIN_CAP = 1u << 17;
#define FAUX 16
constexpr int AUX_SC1 = 16;
#define RDM 0
#define GLM 4
#define WSLEEP 4
#define W2AT 1
#define PSLEEP 6

__device__ __forceinline__ float bf2f(unsigned b) { return __builtin_bit_cast(float, b << 16); }
__device__ __forceinline__ float bfhi(unsigned b) { return __builtin_bit_cast(float, b & 0xffff0000u); }
__device__ __forceinline__ unsigned pkbf(float lo, float hi) {
  v2bf v; v.x = (__bf16)lo; v.y = (__bf16)hi; return __builtin_bit_cast(unsigned, v);
}
__device__ __forceinline__ float rbf(float f) { return bf2f(pkbf(f, 0.f)); }
__device__ __forceinline__ f32x4 mfma(v4u a, v4u b, f32x4 c) {
  return __builtin_amdgcn_mfma_f32_16x16x32_bf16(__builtin_bit_cast(bf16x8, a), __builtin_bit_cast(bf16x8, b), c, 0, 0, 0);
}
__device__ __forceinline__ float dot2(unsigned a, unsigned b, float c) {
  return __builtin_amdgcn_fdot2_f32_bf16(__builtin_bit_cast(v2bf, a), __builtin_bit_cast(v2bf, b), c, false);
}
__device__ __forceinline__ void glds16(const void* g, LAS char* l) {
  __builtin_amdgcn_global_load_lds((const GAS void*)g, (LAS void*)l, 16, 0, 0);
}
__device__ __forceinline__ v2u trread(LAS char* p) {
  return __builtin_bit_cast(v2u, __builtin_amdgcn_ds_read_tr16_b64_v4i16((LAS v4s*)p));
}
__device__ __forceinline__ unsigned silu2(unsigned hb) {
  const f32x2 h = {bf2f(hb), bfhi(hb)};
  const f32x2 t = h * -1.44269504088896f;
  const f32x2 d = f32x2{__builtin_amdgcn_exp2f(t.x), __builtin_amdgcn_exp2f(t.y)} + 1.f;
  const f32x2 y = h * f32x2{__builtin_amdgcn_rcpf(d.x), __builtin_amdgcn_rcpf(d.y)};
  return pkbf(y.x, y.y);
}
struct cplx { float r, i; };
__device__ __forceinline__ float fma_(float a, float b, float c) { float d; asm("v_fma_f32 %0, %1, %2, %3" : "=v"(d) : "v"(a), "v"(b), "v"(c)); return d; }
__device__ __forceinline__ float fnma_(float a, float b, float c) { float d; asm("v_fma_f32 %0, -%1, %2, %3" : "=v"(d) : "v"(a), "v"(b), "v"(c)); return d; }
__device__ __forceinline__ float mul_(float a, float b) { float d; asm("v_mul_f32 %0, %1, %2" : "=v"(d) : "v"(a), "v"(b)); return d; }
__device__ __forceinline__ float add_(float a, float b) { float d; asm("v_add_f32 %0, %1, %2" : "=v"(d) : "v"(a), "v"(b)); return d; }
__device__ __forceinline__ float mul_t(float a, float b) { float d; asm("s_nop 0\n\tv_mul_f32 %0, %1, %2" : "=v"(d) : "v"(a), "v"(b)); return d; }
__device__ __forceinline__ float add_t(float a, float b) { float d; asm("s_nop 0\n\tv_add_f32 %0, %1, %2" : "=v"(d) : "v"(a), "v"(b)); return d; }
__device__ __forceinline__ float silu_f(float h) { return mul_t(h, __builtin_amdgcn_rcpf(add_t(1.f, __builtin_amdgcn_exp2f(mul_(h, -1.44269504088896f))))); }
__device__ __forceinline__ cplx cmul(cplx a, cplx b) { return {fnma_(a.i, b.i, mul_(a.r, b.r)), fma_(a.r, b.i, mul_(a.i, b.r))}; }
__device__ __forceinline__ cplx cfma(cplx a, cplx b, cplx c) {
  return {fma_(a.r, b.r, fnma_(a.i, b.i, c.r)), fma_(a.r, b.i, fma_(a.i, b.r, c.i))};
}
__device__ __forceinline__ unsigned hashA(unsigned a, unsigned b) { unsigned h = a * 0x9E3779B1u + (b ^ (b >> 15)) * 0x85EBCA77u; return h ^ (h >> 13); }
__device__ __forceinline__ unsigned hashB(unsigned a, unsigned b) { unsigned h = b * 0xC2B2AE3Du + (a ^ (a >> 16)) * 0x27D4EB2Fu; return h ^ (h >> 15); }
template <int N> __device__ __forceinline__ void waitv() { asm volatile("s_waitcnt vmcnt(%0)" ::"n"(N) : "memory"); }
#define WAITL0() __builtin_amdgcn_s_waitcnt(0xC07F)
#define BAR() __builtin_amdgcn_s_barrier()
#define SB0() __builtin_amdgcn_sched_barrier(0)
#define CBAR() asm volatile("" ::: "memory")
#define ST_DECL
#define ST(i)
#define SF(i)
#define ST_FLUSH(wsp)

__device__ __forceinline__ unsigned lds_arrive(LAS unsigned* c) {
  unsigned o = 0;
  if ((threadIdx.x & 63) == 0)
    asm volatile("s_waitcnt lgkmcnt(0)\n\tds_add_rtn_u32 %0, %1, %2\n\ts_waitcnt lgkmcnt(0)" : "=&v"(o) : "v"((unsigned)(size_t)c), "v"(1u) : "memory");
  return __builtin_amdgcn_readfirstlane(o);
}
__device__ __forceinline__ void lds_wait(LAS unsigned* c, unsigned want) {
#pragma nounroll
  for (unsigned spins = 0; spins < (1u << 22); ++spins) {
    unsigned v;
    asm volatile("ds_read_b32 %0, %1\n\ts_waitcnt lgkmcnt(0)" : "=&v"(v) : "v"((unsigned)(size_t)c) : "memory");
    if (__builtin_amdgcn_readfirstlane(v) >= want) break;
    __builtin_amdgcn_s_sleep(1);
  }
}
struct Tr14 { v2u h[14]; };
template <int OFF> __device__ __forceinline__ void tr14(Tr14& t, const unsigned (&ad)[7]) {
  asm volatile("ds_read_b64_tr_b16 %0, %14 offset:%21\n\tds_read_b64_tr_b16 %1, %14 offset:%22\n\t"
               "ds_read_b64_tr_b16 %2, %15 offset:%21\n\tds_read_b64_tr_b16 %3, %15 offset:%22\n\t"
               "ds_read_b64_tr_b16 %4, %16 offset:%21\n\tds_read_b64_tr_b16 %5, %16 offset:%22\n\t"
               "ds_read_b64_tr_b16 %6, %17 offset:%21\n\tds_read_b64_tr_b16 %7, %17 offset:%22\n\t"
               "ds_read_b64_tr_b16 %8, %18 offset:%21\n\tds_read_b64_tr_b16 %9, %18 offset:%22\n\t"
               "ds_read_b64_tr_b16 %10, %19 offset:%21\n\tds_read_b64_tr_b16 %11, %19 offset:%22\n\t"
               "ds_read_b64_tr_b16 %12, %20 offset:%21\n\tds_read_b64_tr_b16 %13, %20 offset:%22"
               : "=&v"(t.h[0]), "=&v"(t.h[1]), "=&v"(t.h[2]), "=&v"(t.h[3]), "=&v"(t.h[4]), "=&v"(t.h[5]), "=&v"(t.h[6]),
                 "=&v"(t.h[7]), "=&v"(t.h[8]), "=&v"(t.h[9]), "=&v"(t.h[10]), "=&v"(t.h[11]), "=&v"(t.h[12]), "=&v"(t.h[13])
               : "v"(ad[0]), "v"(ad[1]), "v"(ad[2]), "v"(ad[3]), "v"(ad[4]), "v"(ad[5]), "v"(ad[6]), "n"(OFF), "n"(OFF + 1024)
               : "memory");
}
template <int OFF> __device__ __forceinline__ void rd2x(v4u (&x)[2], unsigned ad) {
  asm volatile("ds_read_b128 %0, %2 offset:%3\n\tds_read_b128 %1, %2 offset:%4" : "=&v"(x[0]), "=&v"(x[1]) : "v"(ad), "n"(OFF), "n"(OFF + 2048) : "memory");
}
template <int N> __device__ __forceinline__ void waitl_pin(Tr14& t, v4u (&x)[2]) {
  asm volatile("s_waitcnt lgkmcnt(%16)"
               : "+v"(t.h[0]), "+v"(t.h[1]), "+v"(t.h[2]), "+v"(t.h[3]), "+v"(t.h[4]), "+v"(t.h[5]), "+v"(t.h[6]), "+v"(t.h[7]), "+v"(t.h[8]),
                 "+v"(t.h[9]), "+v"(t.h[10]), "+v"(t.h[11]), "+v"(t.h[12]), "+v"(t.h[13]), "+v"(x[0]), "+v"(x[1])
               : "n"(N) : "memory");
}
template <int OFF> __device__ __forceinline__ void tr2(v2u& h0, v2u& h1, unsigned ad) {
  asm volatile("ds_read_b64_tr_b16 %0, %2 offset:%3\n\tds_read_b64_tr_b16 %1, %2 offset:%4" : "=&v"(h0), "=&v"(h1) : "v"(ad), "n"(OFF), "n"(OFF + 1024) : "memory");
}
__device__ __forceinline__ void keep(const Tr14& t, const v4u (&x)[2]) {
  asm volatile("" :: "v"(t.h[0]), "v"(t.h[1]), "v"(t.h[2]), "v"(t.h[3]), "v"(t.h[4]), "v"(t.h[5]), "v"(t.h[6]), "v"(t.h[7]), "v"(t.h[8]),
               "v"(t.h[9]), "v"(t.h[10]), "v"(t.h[11]), "v"(t.h[12]), "v"(t.h[13]), "v"(x[0]), "v"(x[1]));
}
__device__ __forceinline__ void waitl_pinS(v4u (&x)[2], v2u (&t)[4]) {
  asm volatile("s_waitcnt lgkmcnt(0)" : "+v"(x[0]), "+v"(x[1]), "+v"(t[0]), "+v"(t[1]), "+v"(t[2]), "+v"(t[3]) :: "memory");
}
template <int OFF> __device__ __forceinline__ void tr4(v2u (&h)[4], unsigned a0, unsigned a1) {
  asm volatile("ds_read_b64_tr_b16 %0, %4 offset:%6\n\tds_read_b64_tr_b16 %1, %4 offset:%7\n\t"
               "ds_read_b64_tr_b16 %2, %5 offset:%6\n\tds_read_b64_tr_b16 %3, %5 offset:%7"
               : "=&v"(h[0]), "=&v"(h[1]), "=&v"(h[2]), "=&v"(h[3]) : "v"(a0), "v"(a1), "n"(OFF), "n"(OFF + 1024) : "memory");
}
template <int OFF> __device__ __forceinline__ void rd4w(v4u (&x)[4], unsigned ad) {
  asm volatile("ds_read_b128 %0, %4 offset:%5\n\tds_read_b128 %1, %4 offset:%6\n\tds_read_b128 %2, %4 offset:%7\n\tds_read_b128 %3, %4 offset:%8"
               : "=&v"(x[0]), "=&v"(x[1]), "=&v"(x[2]), "=&v"(x[3]) : "v"(ad), "n"(OFF), "n"(OFF + 2048), "n"(OFF + 4096), "n"(OFF + 6144) : "memory");
}
template <int OFF> __device__ __forceinline__ void rd1(v4u& x, unsigned ad) {
  asm volatile("ds_read_b128 %0, %1 offset:%2" : "=&v"(x) : "v"(ad), "n"(OFF) : "memory");
}
template <int N> __device__ __forceinline__ void waitl_pin5(v4u (&x)[4], v4u& y) {
  asm volatile("s_waitcnt lgkmcnt(%5)" : "+v"(x[0]), "+v"(x[1]), "+v"(x[2]), "+v"(x[3]), "+v"(y) : "n"(N) : "memory");
}
template <int N> __device__ __forceinline__ void waitl_pin4(v4u (&x)[4]) {
  asm volatile("s_waitcnt lgkmcnt(%4)" : "+v"(x[0]), "+v"(x[1]), "+v"(x[2]), "+v"(x[3]) : "n"(N) : "memory");
}
__device__ __forceinline__ float fdiv(float a, float b) {
  const float rc = __builtin_amdgcn_rcpf(b), q0 = mul_t(a, rc);
  return fma_(fnma_(q0, b, a), rc, q0);
}
typedef std::integral_constant<int, 0> I0;
#define IC(n) std::integral_constant<int, (n)>{}
#define CAS __attribute__((address_space(4)))
__device__ __forceinline__ void wload(unsigned& d, const char* p) { asm volatile("global_load_dword %0, %1, off" : "=&v"(d) : "v"(p) : "memory"); }
struct W8 { v2u h[8]; };
__device__ __forceinline__ void tr2k(v2u& h0, v2u& h1, unsigned ad) {
  asm volatile("ds_read_b64_tr_b16 %0, %2\n\tds_read_b64_tr_b16 %1, %2 offset:2048" : "=&v"(h0), "=&v"(h1) : "v"(ad) : "memory");
}
__device__ __forceinline__ void rd2h(v2u& h0, v2u& h1, unsigned ad) {
  asm volatile("ds_read_b64 %0, %2\n\tds_read_b64 %1, %2 offset:8" : "=&v"(h0), "=&v"(h1) : "v"(ad) : "memory");
}
__device__ __forceinline__ void waitl_pin8(W8& t) {
  asm volatile("s_waitcnt lgkmcnt(0)" : "+v"(t.h[0]), "+v"(t.h[1]), "+v"(t.h[2]), "+v"(t.h[3]), "+v"(t.h[4]), "+v"(t.h[5]), "+v"(t.h[6]), "+v"(t.h[7]) :: "memory");
}
__device__ __forceinline__ void keep8(const W8& t) {
  asm volatile("" :: "v"(t.h[0]), "v"(t.h[1]), "v"(t.h[2]), "v"(t.h[3]), "v"(t.h[4]), "v"(t.h[5]), "v"(t.h[6]), "v"(t.h[7]));
}
__device__ __forceinline__ void pin4(v4u (&x)[4]) { asm volatile("" : "+v"(x[0]), "+v"(x[1]), "+v"(x[2]), "+v"(x[3])); }
template <int OFF> __device__ __forceinline__ void aload1(v4u& r, unsigned voff, const char* sbase) {
  asm volatile("global_load_dwordx4 %0, %1, %2 offset:%3 sc1" : "=&v"(r) : "v"(voff), "s"(sbase), "n"(OFF) : "memory");
}
__device__ __forceinline__ void poll_flags(const char* p, int n, u64 tag) {
  const int lane = threadIdx.x & 63;
  GAS u64* f = (GAS u64*)p + (lane < n ? lane : 0);
#pragma nounroll
  for (unsigned spins = 0; spins < SPIN_CAP; ++spins) {
    if (__all(__hip_atomic_load(f, RLX_AGENT) == tag)) break;
    __builtin_amdgcn_s_sleep(PSLEEP);
  }
  __builtin_amdgcn_fence(__ATOMIC_ACQUIRE, "wavefront");
}
__device__ __forceinline__ void aload4(v4u (&r)[4], unsigned voff, const char* sbase) {
  asm volatile("global_load_dwordx4 %0, %4, %5 sc1\n\tglobal_load_dwordx4 %1, %4, %5 offset:1024 sc1\n\t"
               "global_load_dwordx4 %2, %4, %5 offset:2048 sc1\n\tglobal_load_dwordx4 %3, %4, %5 offset:3072 sc1"
               : "=&v"(r[0]), "=&v"(r[1]), "=&v"(r[2]), "=&v"(r[3]) : "v"(voff), "s"(sbase) : "memory");
}
__device__ __forceinline__ void trread8(v4u (&wf)[4], unsigned a0, unsigned a1, unsigned a2, unsigned a3) {
  v2u x0, x1, x2, x3, x4, x5, x6, x7;
  asm volatile("ds_read_b64_tr_b16 %0, %8\n\tds_read_b64_tr_b16 %1, %8 offset:2048\n\t"
               "ds_read_b64_tr_b16 %2, %9\n\tds_read_b64_tr_b16 %3, %9 offset:2048\n\t"
               "ds_read_b64_tr_b16 %4, %10\n\tds_read_b64_tr_b16 %5, %10 offset:2048\n\t"
               "ds_read_b64_tr_b16 %6, %11\n\tds_read_b64_tr_b16 %7, %11 offset:2048\n\ts_waitcnt lgkmcnt(0)"
               : "=&v"(x0), "=&v"(x1), "=&v"(x2), "=&v"(x3), "=&v"(x4), "=&v"(x5), "=&v"(x6), "=&v"(x7)
               : "v"(a0), "v"(a1), "v"(a2), "v"(a3) : "memory");
  wf[0] = v4u{x0.x, x0.y, x1.x, x1.y}; wf[1] = v4u{x2.x, x2.y, x3.x, x3.y};
  wf[2] = v4u{x4.x, x4.y, x5.x, x5.y}; wf[3] = v4u{x6.x, x6.y, x7.x, x7.y};
}
constexpr int p2_wait(int i, int N, int W, int RD) {
  int total = 0, need = 0;
  for (int j = 0; j < W && j < N; j++) { total += 4; if (j == i) need = total; }
  for (int s = 0; s < i; s++) {
    if (s + RD < N) { total += 4; if (s + RD == i + 1) need = total; }
    if (s + W < N) { total += 4; if (s + W == i) need = total > need ? total : need; }
  }
  return total - need;
}

template <int... I, class Fn> __device__ __forceinline__ void static_for_(std::integer_sequence<int, I...>, Fn&& f) { (f(std::integral_constant<int, I>{}), ...); }
template <int N, class Fn> __device__ __forceinline__ void static_for(Fn&& f) { static_for_(std::make_integer_sequence<int, N>{}, f); }

struct Args {
  const u16 *x, *Lre, *Lim, *Bre, *Bim, *Cre, *Cim, *logdt, *Dsk, *w1, *w2;
  char* ws; float* out;
};

template <bool FFN>
__device__ __forceinline__ void body(const Args& a, LAS char* L, const int w, v2u ctouch) {
  const int b = blockIdx.x, xcd = b & 7, idx = b >> 3;
  const int r = (xcd & 1) * 8 + (idx & 7), c = (xcd >> 1) * 4 + (idx >> 3);
  const int rt = 4 * xcd + (idx & 3), ct = idx >> 2;
  const int tid = threadIdx.x, lane = tid & 63, tok = lane & 15, g = lane >> 4, tg = w & 3;
  const int codd = c & 1;
  char* const ws = a.ws;
  LAS unsigned* CNT = (LAS unsigned*)(L + M_CNT);
  ST_DECL ST(0);
  const __amdgpu_buffer_rsrc_t rsw = __builtin_amdgcn_make_buffer_rsrc((void*)ws, 0, (int)WS_END, 0x00020000);
  const int r8 = lane >> 3, p8 = lane & 7;
  constexpr int N2 = FFN ? 14 : 8;
  const int s0 = FFN ? 14 * tg : 56 + 8 * tg;
  const char* bw0 = (const char*)(a.w2 + 64 * ct);
  const unsigned bwl = (r8 * D_ + (((((p8 >> 1) ^ ((r8 >> 1) & 3)) << 1) | (p8 & 1)) << 3)) * 2;
  const int r4 = lane >> 2, c4 = lane & 3;
  const size_t bc0 = (size_t)(64 * ct) * 1024;
  const unsigned bcl = (r4 * 512 + ((c4 ^ ((-(r4 >> 2)) & 3)) << 3)) * 2;
  constexpr int RD = FFN ? 3 : 5, WA = FFN ? 4 : 7;
  auto ringp = [&](int i) {
    const int e = i % RD;
    return L + (FFN ? 65536 + tg * 12288 + e * 4096 : e == 0 ? 114688 + tg * 4096 : e < 3 ? 32768 + tg * 8192 + (e - 1) * 4096 : tg * 8192 + (e - 3) * 4096);
  };
  auto srcF = [&](int f, int i, int j) { return bw0 + (size_t)(32 * (14 * f + i) + 8 * j) * (D_ * 2) + bwl; };
  auto srcS = [&](int f, int i, int j) { return (const char*)((i & 1) ? a.Cim : a.Cre) + bc0 + 256 * f + 64 * (i >> 1) + (size_t)j * 16 * 1024 + bcl; };
  auto issueB = [&](int i) {
    LAS char* sl = ringp(i);
#pragma unroll
    for (int j = 0; j < 4; j++) glds16(FFN ? srcF(tg, i, j) : srcS(tg, i, j), sl + j * 1024);
  };
  const int swz = (tok >> 1) & 7;
  const int xoff = (32 * tg + tok) * 128 + ((g ^ swz) << 4);
  const int q = tok >> 2, p4 = lane & 3, fl = q | ((g & 1) << 2);
  const unsigned lb = (unsigned)(size_t)L;
  const unsigned xad[4] = {lb + xoff, lb + (xoff ^ 64), lb + xoff + 2 * SLOT, lb + (xoff ^ 64) + 2 * SLOT};
  auto waddr = [&](int v) { const int pi = codd ? (v == 6 ? 1 : v + 2) : v; return lb + OFF_W + (8 * g + q) * 256 + ((pi ^ fl) << 5) + p4 * 8; };
  constexpr unsigned ONES = 0x3f803f80u;
  LAS f32x2* STT = (LAS f32x2*)(L + M_STT);
  LAS float* CS = (LAS float*)(L + M_CS);
  u64 tag;

  if (FFN) {
    tag = *(const CAS u64*)(ws + WS_EP) + 1ull;
    if (tid < 16) CNT[tid] = 0u;
    if (w == 0 && lane < 32) {
      const float lr = bf2f(a.Lre[32 * c + lane]), li = bf2f(a.Lim[32 * c + lane]), dt = rbf(expf(bf2f(a.logdt[32 * c + lane])));
      const float er = expf(dt * lr); float sn, cn; sincosf(dt * li, &sn, &cn);
      const float br = er * cn, bi = er * sn, den = 1.f / (lr * lr + li * li), nr = br - 1.f;
      *(LAS f32x4*)(L + M_PRM + lane * 16) = f32x4{br, bi, (nr * lr + bi * li) * den, (bi * lr - nr * li) * den};
    }
    unsigned warm_ = 0, warm2_ = 0;
    if (tid < 152) {
      const int t = b * 152 + tid;
      const char* p = t < 14336 ? (const char*)a.w1 + (size_t)t * 128
                    : t < 30720 ? (const char*)a.x + (size_t)(t - 14336) * 128
                    : t < 34816 ? (const char*)a.Bre + (size_t)(t - 30720) * 128 : (const char*)a.Bim + (size_t)(t - 34816) * 128;
      __builtin_amdgcn_s_sleep(WSLEEP);
      asm volatile("global_load_dword %0, %1, off" : "=&v"(warm_) : "v"(p) : "memory");
    }
    unsigned warm5_ = 0, warm6_ = 0;
    {
      const char* p0 = tid < 180 ? ws + (size_t)tid * 65536 : tid < 244 ? (const char*)a.out + (size_t)(tid - 180) * 65536 : (const char*)a.Cre + (size_t)((tid - 244) & 7) * 65536;
      const char* p1 = lane < 28 ? (const char*)a.w2 + (size_t)lane * 65536 : (const char*)a.Cim + (size_t)(lane & 7) * 65536;
      wload(warm5_, p0);
      if (tg == 3) wload(warm6_, p1);
    }
    const int t2 = b * 88 + (tid < 88 ? tid : 0);
    const char* wp2 = t2 < 14336 ? (const char*)a.w2 + (size_t)t2 * 128
                    : t2 < 18432 ? (const char*)a.Cre + (size_t)(t2 - 14336) * 128 : (const char*)a.Cim + (size_t)(t2 - 18432) * 128;
    unsigned wlo[7], whi[7];
#pragma unroll
    for (int v = 0; v < 7; v++) { wlo[v] = waddr(v); whi[v] = wlo[v] + 2 * SLOT; }
    f32x4 acc[7][2];
#pragma unroll
    for (int u = 0; u < 7; u++) { acc[u][0] = f32x4{0, 0, 0, 0}; acc[u][1] = f32x4{0, 0, 0, 0}; }
    v4u xf[2][2]; Tr14 wt[2];
    auto rd = [&](auto kc) {
      constexpr int ks = decltype(kc)::value, kk = ks & 1, sl = (ks >> 1) & 3;
      rd2x<(sl & 1) * SLOT>(xf[kk], xad[(sl >> 1) * 2 + kk]);
      tr14<(sl & 1) * SLOT + kk * 8192>(wt[kk], (sl >> 1) ? whi : wlo);
    };
    auto step = [&](auto kc) {
      constexpr int ks = decltype(kc)::value, kk = ks & 1, kn = ks + 1, nk = kn & 1, sl = (kn >> 1) & 3;
      static_for<14>([&](auto mc) {
        constexpr int m = decltype(mc)::value, v = m >> 1, tt = m & 1;
        const v4u wv = {wt[kk].h[2 * v].x, wt[kk].h[2 * v].y, wt[kk].h[2 * v + 1].x, wt[kk].h[2 * v + 1].y};
        acc[v][tt] = mfma(wv, xf[kk][tt], acc[v][tt]);
        SB0();
        if constexpr (kn < 16) {
          if (m == 0) rd2x<(sl & 1) * SLOT>(xf[nk], xad[(sl >> 1) * 2 + nk]);
          if constexpr (m >= 1 && m <= 7) tr2<(sl & 1) * SLOT + nk * 8192>(wt[nk].h[2 * (m - 1)], wt[nk].h[2 * (m - 1) + 1], (sl >> 1) ? whi[m - 1] : wlo[m - 1]);
          SB0();
        }
      });
      keep(wt[kk], xf[kk]);
      if constexpr (kn < 16) waitl_pin<0>(wt[nk], xf[nk]);
    };
    WAITL0(); BAR();
    ST(1);
    rd(IC(0));
    waitl_pin<0>(wt[0], xf[0]);
    static_for<8>([&](auto sc) {
      constexpr int s = decltype(sc)::value;
      SF(1);
      step(IC(2 * s));
      SF(5);
      BAR();
      SF(6);
      if (s == 3) ST(2);
      if (s == W2AT && tg < 2) wload(warm2_, wp2);
      step(IC(2 * s + 1));
      SF(8);
    });
    asm volatile("s_waitcnt vmcnt(0)" : "+v"(warm_), "+v"(warm2_), "+v"(warm5_), "+v"(warm6_), "+v"(ctouch) :: "memory");
    ST(3);
    BAR();
    ST(4);
    {
      __builtin_amdgcn_s_setprio(3);
      const f32x2 st0 = STT[32 * tg + tok], st1 = STT[32 * tg + 16 + tok];
      f32x4 cs[7];
#pragma unroll
      for (int v = 0; v < 7; v++) cs[v] = *(LAS f32x4*)(CS + v * 16 + 4 * g);
      const int T16 = 8 * r + 2 * tg;
      const int fb = (int)WS_H + ((T16 >> 2) * KS2 * 4 + (T16 & 3)) * 1024 + lane * 16;
      const float km[2] = {-st0.x * st0.y, -st1.x * st1.y}, rs[2] = {st0.y, st1.y};
      v2u o[2][2];
#pragma unroll
      for (int v = 0; v < 7; v++) {
#pragma unroll
        for (int tt = 0; tt < 2; tt++) {
          unsigned pk[2];
#pragma unroll
          for (int hh = 0; hh < 2; hh++) {
            const f32x2 ac = {acc[v][tt][2 * hh], acc[v][tt][2 * hh + 1]}, cc = {cs[v][2 * hh], cs[v][2 * hh + 1]};
            const f32x2 u = __builtin_elementwise_fma(ac, f32x2{rs[tt], rs[tt]}, cc * km[tt]);
            unsigned hb = pkbf(u.x, u.y);
            asm volatile("" : "+v"(hb));
            pk[hh] = silu2(hb);
          }
          o[v & 1][tt] = v2u{pk[0], pk[1]};
        }
        if (v & 1) {
          const int U = 7 * c + v - 1 + codd;
#pragma unroll
          for (int tt = 0; tt < 2; tt++)
            __builtin_amdgcn_raw_buffer_store_b128(v4u{o[0][tt].x, o[0][tt].y, o[1][tt].x, o[1][tt].y}, rsw, fb + tt * 1024 + (U >> 1) * 4096, 0, FAUX);
        } else if (v == 6) {
          const int U = codd ? 7 * c : 7 * c + 6;
#pragma unroll
          for (int tt = 0; tt < 2; tt++)
            __builtin_amdgcn_raw_buffer_store_b64(o[0][tt], rsw, fb + tt * 1024 + (U >> 1) * 4096 + (U & 1) * 8, 0, FAUX);
        }
      }
    }
    ST(5);
    lds_arrive(CNT + 7);
    waitv<0>();
    if (lds_arrive(CNT + 3) == 3u) {
      lds_wait(CNT + 0, 4u);
      if (lane == 0) __hip_atomic_store((GAS u64*)(ws + WS_FL + r * 256 + c * 8), tag, RLX_AGENT);
    }
    ST(6);
    __builtin_amdgcn_s_setprio(0);
  } else {
    unsigned sx[2], sw[2];
#pragma unroll
    for (int h = 0; h < 2; h++) {
      sx[h] = ((8 * h + r8) * D_ + ((p8 ^ ((4 * h + (r8 >> 1)) & 7)) << 3)) * 2;
      sw[h] = ((8 * h + g) * F_ + (((((tok >> 1) ^ (g | (h << 2))) << 1) | (tok & 1)) << 3)) * 2;
    }
    const char* xb = (const char*)(a.x + (size_t)(128 * r + 32 * tg) * D_);
    const char* wb1 = (const char*)(a.w1 + (size_t)(16 * tg) * F_ + 112 * c - 16 * codd);
    const int rot = idx & 7;
    auto issue1 = [&](int s, int j) {
      LAS char* sb = L + (s & 3) * SLOT;
      const int k0 = ((s + rot) & 7) * 64;
      if (j < 4) glds16(xb + ((j >> 1) * 16 * D_ + k0) * 2 + sx[j & 1], sb + (4 * tg + j) * 1024);
      else glds16(wb1 + (size_t)(k0 + (j & 1) * 4) * F_ * 2 + sw[(j - 4) >> 1], sb + OFF_W + (4 * tg + j - 4) * 1024);
    };
    auto issue = [&](int s) {
#pragma unroll
      for (int j = 0; j < 8; j++) issue1(s, j);
    };
    issue(0); issue(1);
    __builtin_amdgcn_s_setprio(3);
    tag = *(const CAS u64*)(ws + WS_EP) + 1ull;
    const u16* ssp[2];
#pragma unroll
    for (int h = 0; h < 2; h++) {
      const int n = 16 * tg + 8 * h + r8;
      ssp[h] = ((n & 1) ? a.Bim : a.Bre) + (size_t)(32 * c + (n >> 1)) * D_ + ((p8 ^ ((n >> 1) & 7)) << 3);
    }
    auto issueS1 = [&](int js, int j) { glds16(ssp[j & 1] + ((4 * js + (j >> 1) + rot) & 7) * 64, L + js * SLOT + (j >> 1) * 8192 + (2 * tg + (j & 1)) * 1024); };
    auto issueR1 = [&](int i, int j) {
      const int o = 32 * i + 8 * tg + j;
      if (o < 48) glds16(srcF(o / 12, (o % 12) >> 2, o & 3), L + 65536 + o * 1024);
      else glds16(srcS((o - 48) >> 2, 0, o & 3), L + 65536 + o * 1024);
    };
    auto piece = [&](int s, int j) {
      if (s <= 3) issue1(s + 4, j); else if (s <= 5) issueS1(s - 4, j); else issueR1(s - 6, j);
    };
    const unsigned wa0 = waddr(tg), wa1 = waddr(tg == 3 ? 3 : tg + 4);
    const v4u ones = {ONES, ONES, ONES, ONES};
    v4u xr[16][2];
    v2u wq[2][4];
    f32x4 sm[2] = {{0, 0, 0, 0}, {0, 0, 0, 0}}, sq[2] = {{0, 0, 0, 0}, {0, 0, 0, 0}}, cf[2] = {{0, 0, 0, 0}, {0, 0, 0, 0}};
    auto rdS = [&](auto kc) {
      constexpr int ks = decltype(kc)::value, kk = ks & 1, sl = (ks >> 1) & 3;
      rd2x<(sl & 1) * SLOT>(xr[ks], xad[(sl >> 1) * 2 + kk]);
      tr4<(sl & 1) * SLOT + kk * 8192>(wq[kk], wa0 + (sl >> 1) * 2 * SLOT, wa1 + (sl >> 1) * 2 * SLOT);
    };
    auto pinS = [&](auto kc) {
      constexpr int ks = decltype(kc)::value, kk = ks & 1;
      waitl_pinS(xr[ks], wq[kk]);
    };
    auto stat = [&](auto kc, int s, int j0) {
      constexpr int ks = decltype(kc)::value, kk = ks & 1;
      sm[0] = mfma(ones, xr[ks][0], sm[0]); sq[0] = mfma(xr[ks][0], xr[ks][0], sq[0]);
      SB0(); if (j0 >= 0) piece(s, j0); SB0();
      sm[1] = mfma(ones, xr[ks][1], sm[1]);
      SB0(); if (j0 >= 0) piece(s, j0 + 1); SB0();
      sq[1] = mfma(xr[ks][1], xr[ks][1], sq[1]);
      SB0(); if (j0 >= 0) piece(s, j0 + 2); SB0();
      cf[0] = mfma(v4u{wq[kk][0].x, wq[kk][0].y, wq[kk][1].x, wq[kk][1].y}, ones, cf[0]);
      SB0(); if (j0 >= 0) piece(s, j0 + 3); SB0();
      cf[1] = mfma(v4u{wq[kk][2].x, wq[kk][2].y, wq[kk][3].x, wq[kk][3].y}, ones, cf[1]);
      SB0();
    };
    const int sch = lane & 7, seg = lane >> 3, chg = 32 * c + 8 * tg + sch;
    cplx lam, coef, l16, l32, l64, lseg = {1.f, 0.f}, wa = {0.f, 0.f}, wb = {0.f, 0.f};
    waitv<8>();
    BAR();
    ST(1);
    rdS(IC(0));
    issue(2);
    pinS(IC(0));
    static_for<8>([&](auto sc) {
      constexpr int s = decltype(sc)::value;
      SF(1);
      rdS(IC(2 * s + 1));
      if (s == 0) { stat(IC(0), -1, 0); issue1(3, 4); issue1(3, 5); issue1(3, 6); issue1(3, 7); }
      else stat(IC(2 * s), s - 1, 4);
      pinS(IC(2 * s + 1));
      SF(2);
      if (s <= 6) waitv<16>();
      SF(3);
      BAR();
      SF(6);
      if (s == 3) ST(2);
      if constexpr (s < 7) rdS(IC(2 * s + 2));
      stat(IC(2 * s + 1), s, 0);
      if constexpr (s < 7) pinS(IC(2 * s + 2));
      SF(8);
      if (s == 1) {
        const f32x4 pr = *(LAS f32x4*)(L + M_PRM + (8 * tg + sch) * 16);
        lam = {pr[0], pr[1]}; coef = {pr[2], pr[3]};
        const cplx l2 = cmul(lam, lam), l4 = cmul(l2, l2), l8 = cmul(l4, l4);
        l16 = cmul(l8, l8); l32 = cmul(l16, l16); l64 = cmul(l32, l32);
        const cplx l128 = cmul(l64, l64), l256 = cmul(l128, l128), l512 = cmul(l256, l256), l1024 = cmul(l512, l512);
        if (seg & 1) lseg = l16;
        if (seg & 2) lseg = cmul(lseg, l32);
        if (seg & 4) lseg = cmul(lseg, l64);
        const int nb = r - 1 - (seg + 8);
        cplx t = {1.f, 0.f};
        const int ne = nb >= 0 ? nb : (nb + 8 >= 0 ? nb + 8 : 0);
        if (ne & 1) t = l128;
        if (ne & 2) t = cmul(t, l256);
        if (ne & 4) t = cmul(t, l512);
        if (nb >= 0) { wb = t; wa = cmul(t, l1024); } else if (nb + 8 >= 0) { wa = t; }
        asm volatile("" : "+v"(wa.r), "+v"(wa.i), "+v"(wb.r), "+v"(wb.i), "+v"(lseg.r), "+v"(lseg.i));
      }
    });
    {
      const int e = tok & 3;
      const float d0 = e == 0 ? sq[0][0] : e == 1 ? sq[0][1] : e == 2 ? sq[0][2] : sq[0][3];
      const float d1 = e == 0 ? sq[1][0] : e == 1 ? sq[1][1] : e == 2 ? sq[1][2] : sq[1][3];
      const bool own = g == q;
#pragma unroll
      for (int tt = 0; tt < 2; tt++) {
        float m = sm[tt][0] * (1.f / 512.f);
        asm volatile("" : "+v"(m));
        const float var = fmaxf(fnma_(m, m, mul_(tt ? d1 : d0, 1.f / 512.f)), 0.f);
        const float mb = rbf(m), sd = rbf(__builtin_amdgcn_sqrtf(rbf(rbf(var) + 1e-5f)));
        if (own) {
          STT[32 * tg + 16 * tt + tok] = f32x2{mb, fdiv(1.f, sd)};
          if (c == 0)
            __builtin_amdgcn_raw_buffer_store_b64(v2u{__builtin_bit_cast(unsigned, mb), __builtin_bit_cast(unsigned, sd)}, rsw,
                                                  (int)WS_ST + (128 * r + 32 * tg + 16 * tt + tok) * 8, 0, AUX_SC1);
        }
      }
      if (tok == 0) {
        *(LAS f32x4*)(CS + tg * 16 + 4 * g) = cf[0];
        if (tg < 3) *(LAS f32x4*)(CS + (tg + 4) * 16 + 4 * g) = cf[1];
      }
    }
    ST(3);
    WAITL0(); BAR();
    ST(4);
#pragma unroll
    for (int j = 4; j < 8; j++) piece(7, j);
    __builtin_amdgcn_s_setprio(0);
    f32x4 acc[4][2];
    f32x4 cs4 = {0, 0, 0, 0};
#pragma unroll
    for (int u = 0; u < 4; u++) { acc[u][0] = f32x4{0, 0, 0, 0}; acc[u][1] = f32x4{0, 0, 0, 0}; }
    const unsigned sad[2] = {lb + tok * 128 + ((g ^ swz) << 4), lb + ((tok * 128 + ((g ^ swz) << 4)) ^ 64)};
    v4u wf[3][4], wo[3];
    const unsigned oad[2] = {sad[0] + tg * 2048, sad[1] + tg * 2048};
    waitv<0>();
    lds_arrive(CNT + 0); lds_wait(CNT + 0, 4u);
    rd4w<0>(wf[0], sad[0]); rd1<0>(wo[0], oad[0]);
    rd4w<0>(wf[1], sad[1]); rd1<0>(wo[1], oad[1]);
    lds_wait(CNT + 7, 4u);
    static_for<16>([&](auto kc) {
      constexpr int ks = decltype(kc)::value, kn = ks + 2;
      if constexpr (kn < 16) {
        rd4w<(kn >> 3) * SLOT + ((kn >> 1) & 3) * 8192>(wf[kn % 3], sad[kn & 1]);
        rd1<(kn >> 3) * SLOT + ((kn >> 1) & 3) * 8192>(wo[kn % 3], oad[kn & 1]);
        waitl_pin5<10>(wf[ks % 3], wo[ks % 3]);
      } else if constexpr (kn == 16) waitl_pin5<5>(wf[ks % 3], wo[ks % 3]);
      else waitl_pin5<0>(wf[ks % 3], wo[ks % 3]);
      SB0();
#pragma unroll
      for (int u = 0; u < 4; u++) { acc[u][0] = mfma(wf[ks % 3][u], xr[ks][0], acc[u][0]); acc[u][1] = mfma(wf[ks % 3][u], xr[ks][1], acc[u][1]); }
      cs4 = mfma(wo[ks % 3], ones, cs4);
      if (ks == 8) lds_arrive(CNT + 1);
      SB0();
    });
    ST(5);
    __builtin_amdgcn_s_setprio(3);
    lds_wait(CNT + 1, 4u);
#pragma unroll
    for (int u = 0; u < 4; u++)
#pragma unroll
      for (int tt = 0; tt < 2; tt++) {
        const int token = 32 * tg + 16 * tt + tok;
        *(LAS f32x4*)(L + token * 256 + (((4 * u + g) ^ (token & 7) ^ (((token >> 4) & 3) << 2)) << 4)) = acc[u][tt];
      }
    const float ca = lane & 1 ? cs4[2] : cs4[0], cb = lane & 1 ? cs4[3] : cs4[1];
    const f32x2 c2 = {__shfl(ca, (sch >> 1) * 16 + (sch & 1)), __shfl(cb, (sch >> 1) * 16 + (sch & 1))};
    lds_arrive(CNT + 2);
    lds_wait(CNT + 2, 4u);
    cplx bu[16], h = {0.f, 0.f}, init0 = {0.f, 0.f};
    asm volatile("" : "+v"(l16.r), "+v"(l16.i));
    l32 = cmul(l16, l16); l64 = cmul(l32, l32);
    {
      const int rbase = seg * 4096 + (sch & 1) * 8, rch = (4 * tg + (sch >> 1)) ^ ((seg & 3) << 2);
      f32x2 v[16]; f32x4 st[8];
#pragma unroll
      for (int t = 0; t < 16; t++) v[t] = *(LAS f32x2*)(L + rbase + t * 256 + ((rch ^ (t & 7)) << 4));
#pragma unroll
      for (int t = 0; t < 8; t++) st[t] = *(LAS f32x4*)(L + M_STT + (16 * seg + 2 * t) * 8);
#pragma unroll
      for (int t = 0; t < 16; t++) {
        const float mu = st[t >> 1][(t & 1) * 2], rs = st[t >> 1][(t & 1) * 2 + 1];
        bu[t] = {mul_(fnma_(mu, c2.x, v[t].x), rs), mul_(fnma_(mu, c2.y, v[t].y), rs)};
        h = cfma(lam, h, bu[t]);
      }
      { cplx o = {__shfl_up(h.r, 8), __shfl_up(h.i, 8)}; if (seg >= 1) h = cfma(l16, o, h); }
      { cplx o = {__shfl_up(h.r, 16), __shfl_up(h.i, 16)}; if (seg >= 2) h = cfma(l32, o, h); }
      { cplx o = {__shfl_up(h.r, 32), __shfl_up(h.i, 32)}; if (seg >= 4) h = cfma(l64, o, h); }
      if (seg == 7 && r < 15) {
        const unsigned ur = __builtin_bit_cast(unsigned, h.r), ui = __builtin_bit_cast(unsigned, h.i);
        GAS u64* gp = (GAS u64*)(ws + WS_E + ((size_t)r * 512 + chg) * 16);
        __hip_atomic_store(gp, ((u64)((unsigned)tag + hashA(ur, ui)) << 32) | ur, RLX_AGENT);
        __hip_atomic_store(gp + 1, ((u64)((unsigned)(tag >> 32) + hashB(ur, ui)) << 32) | ui, RLX_AGENT);
      }
      { cplx o = {__shfl_up(h.r, 8), __shfl_up(h.i, 8)}; if (seg >= 1) init0 = o; }
    }
    ST(6);
    lds_arrive(CNT + 5);
    issueB(1); issueB(2);
    cplx carry = {0.f, 0.f};
    if (r > 0) {
      const bool needA = seg < r, needB = seg + 8 < r;
      GAS u64* ga = (GAS u64*)(ws + WS_E + ((size_t)seg * 512 + chg) * 16);
      GAS u64* gb = ga + (size_t)8 * 512 * 2;
      u64 a0 = 0, a1 = 0, b0 = 0, b1 = 0;
#pragma nounroll
      for (unsigned spins = 0; spins < SPIN_CAP; ++spins) {
        bool ok = true;
        if (needA) { a0 = __hip_atomic_load(ga, RLX_AGENT); a1 = __hip_atomic_load(ga + 1, RLX_AGENT); }
        if (needB) { b0 = __hip_atomic_load(gb, RLX_AGENT); b1 = __hip_atomic_load(gb + 1, RLX_AGENT); }
        if (needA) ok = (unsigned)(a0 >> 32) == (unsigned)tag + hashA((unsigned)a0, (unsigned)a1) &&
                        (unsigned)(a1 >> 32) == (unsigned)(tag >> 32) + hashB((unsigned)a0, (unsigned)a1);
        if (needB) ok = ok && (unsigned)(b0 >> 32) == (unsigned)tag + hashA((unsigned)b0, (unsigned)b1) &&
                        (unsigned)(b1 >> 32) == (unsigned)(tag >> 32) + hashB((unsigned)b0, (unsigned)b1);
        if (__all(ok)) break;
        __builtin_amdgcn_s_sleep(2);
      }
      const cplx ea = {__builtin_bit_cast(float, (unsigned)a0), __builtin_bit_cast(float, (unsigned)a1)};
      const cplx eb = {__builtin_bit_cast(float, (unsigned)b0), __builtin_bit_cast(float, (unsigned)b1)};
      cplx s1 = {0.f, 0.f};
      if (needA) s1 = cmul(wa, ea);
      if (needB) s1 = cfma(wb, eb, s1);
      s1.r = add_(s1.r, __shfl_xor(s1.r, 8)); s1.i = add_(s1.i, __shfl_xor(s1.i, 8));
      s1.r = add_(s1.r, __shfl_xor(s1.r, 16)); s1.i = add_(s1.i, __shfl_xor(s1.i, 16));
      s1.r = add_(s1.r, __shfl_xor(s1.r, 32)); s1.i = add_(s1.i, __shfl_xor(s1.i, 32));
      carry = s1;
    }
    ST(7);
    h = cfma(lseg, carry, init0);
    LAS char* tr = L + M_TR + tg * 4224;
#pragma unroll
    for (int t = 0; t < 16; t++) {
      h = cfma(lam, h, bu[t]);
      const cplx o = cmul(coef, h);
      const unsigned pk = pkbf(o.r, -o.i);
      LAS u16* dst = (LAS u16*)(tr + seg * 528 + t * 32 + sch * 2);
      dst[0] = (u16)pk; dst[8] = (u16)(pk >> 16);
    }
    CBAR();
#pragma unroll
    for (int j = 0; j < 4; j++) {
      const int part = j & 1, token = (j >> 1) * 64 + lane;
      const v4u v = *(LAS v4u*)(tr + (token >> 4) * 528 + (token & 15) * 32 + part * 16);
      const int T16 = 8 * r + (token >> 4);
      __builtin_amdgcn_raw_buffer_store_b128(v, rsw, (int)WS_H + (((T16 >> 2) * KS2 + 56 + 2 * c + part) * 4 + (T16 & 3)) * 1024 + ((token & 15) + 16 * tg) * 16, 0, AUX_SC1);
    }
    waitv<0>();
    if (lds_arrive(CNT + 4) == 3u && lane == 0) __hip_atomic_store((GAS u64*)(ws + WS_FL + r * 256 + 128 + c * 8), tag, RLX_AGENT);
    __builtin_amdgcn_s_setprio(0);
    lds_wait(CNT + 5, 4u);
    issueB(3); issueB(4);
  }
  ST(8);
  v2u ex[4], ed; unsigned es[4][2];
  if (FFN) {
#pragma unroll
    for (int j = 0; j < 4; j++) ex[j] = *(const v2u*)(a.x + (size_t)(64 * rt + 16 * j + (tid >> 4)) * D_ + 64 * ct + 4 * (tid & 15));
    ed = *(const v2u*)(a.Dsk + 64 * ct + 4 * (tid & 15));
  }
  if (FFN) {
    if (tg == 0) { poll_flags(ws + WS_FL + (rt >> 1) * 256, 16, tag); lds_arrive(CNT + 6); }
    else lds_wait(CNT + 6, 1u);
  }
  else poll_flags(ws + WS_FL + (rt >> 1) * 256 + 128 + tg * 32, 4, tag);
  ST(9);
  const char* ap = ws + WS_H + ((size_t)rt * KS2 + s0) * 4096;
  if (FFN) {
#pragma unroll
    for (int j = 0; j < 4; j++) {
      const v2u t = __builtin_amdgcn_raw_buffer_load_b64(rsw, (int)WS_ST + (64 * rt + 16 * j + (tid >> 4)) * 8, 0, AUX_SC1);
      es[j][0] = t.x; es[j][1] = t.y;
    }
  }
  v4u ra[WA + 1][4];
#pragma unroll
  for (int i = 0; i < WA; i++) aload4(ra[i], lane * 16, ap + (size_t)i * 4096);
  if (FFN) lds_wait(CNT + 0, 4u);
  const int fw = (2 * g + (q >> 1)) & 3;
  unsigned toff[4];
#pragma unroll
  for (int j = 0; j < 4; j++) toff[j] = FFN ? (4 * g + q) * 128 + ((j ^ fw) << 5) + p4 * 8 : tok * 64 + ((g ^ ((-(tok >> 2)) & 3)) << 4) + j * 1024;
  f32x4 acc2[4][4];
#pragma unroll
  for (int i = 0; i < 4; i++)
#pragma unroll
    for (int j = 0; j < 4; j++) acc2[i][j] = f32x4{0, 0, 0, 0};
  W8 wf[2];
  auto rdw = [&](int i, int j) {
    const unsigned ad = (unsigned)(size_t)ringp(i) + toff[j];
    if (FFN) tr2k(wf[i & 1].h[2 * j], wf[i & 1].h[2 * j + 1], ad); else rd2h(wf[i & 1].h[2 * j], wf[i & 1].h[2 * j + 1], ad);
  };
#pragma unroll
  for (int j = 0; j < 4; j++) rdw(0, j);
  waitl_pin8(wf[0]);
  static_for<N2>([&](auto ic) {
    constexpr int i = decltype(ic)::value;
    waitv<p2_wait(i, N2, WA, RD)>();
    pin4(ra[i % (WA + 1)]);
    if (i == 0) ST(10);
    if (i == 4) ST(11);
    SB0();
    static_for<16>([&](auto mc) {
      constexpr int m = decltype(mc)::value, j = m >> 2, t = m & 3;
      const v4u wv = {wf[i & 1].h[2 * j].x, wf[i & 1].h[2 * j].y, wf[i & 1].h[2 * j + 1].x, wf[i & 1].h[2 * j + 1].y};
      acc2[j][t] = mfma(wv, ra[i % (WA + 1)][t], acc2[j][t]);
      SB0();
      if constexpr (m >= RDM && m < RDM + 4 && i + 1 < N2) rdw(i + 1, m - RDM);
      if constexpr (m >= GLM && m < GLM + 4 && i + RD < N2) glds16(FFN ? srcF(tg, i + RD, m - GLM) : srcS(tg, i + RD, m - GLM), ringp(i + RD) + (m - GLM) * 1024);
      if constexpr (m >= 8 && m < 12 && i + WA < N2) aload1<(m - 8) * 1024>(ra[(i + WA) % (WA + 1)][m - 8], lane * 16, ap + (size_t)(i + WA) * 4096);
      SB0();
    });
    keep8(wf[i & 1]);
    if constexpr (i + 1 < N2) waitl_pin8(wf[(i + 1) & 1]);
  });
  ST(12);
  auto pieceF = [&](int f, int t) { return L + (t < 3 ? 65536 + f * 12288 + t * 4096 : M_TR + f * 4096); };
  auto pieceS = [&](int f, int t) { return L + (t == 0 ? 114688 + f * 4096 : t < 3 ? 32768 + f * 8192 + (t - 1) * 4096 : f * 8192); };
  if (FFN) lds_wait(CNT + 4, 4u);
#pragma unroll
  for (int t = 0; t < 4; t++)
#pragma unroll
    for (int j = 0; j < 4; j++)
      *(LAS f32x4*)((FFN ? pieceF(tg, t) : pieceS(tg, t)) + (j * 4 + g) * 256 + ((tok + 4 * j + g) & 15) * 16) = acc2[j][t];
  lds_arrive(CNT + (FFN ? 8 : 9));
  if (!FFN) {
    if (b == 255 && w == 7) {
      poll_flags(ws + WS_FL + 15 * 256 + 128, 16, tag);
      if (lane == 0) __hip_atomic_store((GAS u64*)(ws + WS_EP), tag, RLX_AGENT);
    }
  } else {
    const int po = (tid & 15) * 256 + (((tid >> 4) + (tid & 15)) & 15) * 16;
    f32x4 sk[4], gg[4];
#pragma unroll
    for (int j = 0; j < 4; j++)
#pragma unroll
      for (int e = 0; e < 4; e++) {
        const unsigned xw = e < 2 ? ex[j].x : ex[j].y, dw = e < 2 ? ed.x : ed.y;
        const float xv = (e & 1) ? bfhi(xw) : bf2f(xw), dv = (e & 1) ? bfhi(dw) : bf2f(dw);
        sk[j][e] = rbf(mul_(rbf(fdiv(rbf(add_(xv, -__builtin_bit_cast(float, es[j][0]))), __builtin_bit_cast(float, es[j][1]))), dv));
      }
    lds_wait(CNT + 8, 4u);
#pragma unroll
    for (int j = 0; j < 4; j++) {
      const f32x4 p0 = *(LAS f32x4*)(pieceF(0, j) + po), p1 = *(LAS f32x4*)(pieceF(1, j) + po), p2 = *(LAS f32x4*)(pieceF(2, j) + po), p3 = *(LAS f32x4*)(pieceF(3, j) + po);
#pragma unroll
      for (int e = 0; e < 4; e++) gg[j][e] = rbf(silu_f(rbf(add_(add_(p0[e], p1[e]), add_(p2[e], p3[e])))));
    }
    lds_wait(CNT + 9, 4u);
    ST(13);
#pragma unroll
    for (int j = 0; j < 4; j++) {
      const f32x4 sy = (*(LAS f32x4*)(pieceS(0, j) + po) + *(LAS f32x4*)(pieceS(1, j) + po)) + (*(LAS f32x4*)(pieceS(2, j) + po) + *(LAS f32x4*)(pieceS(3, j) + po));
      const f32x4 xv = {bf2f(ex[j].x), bfhi(ex[j].x), bf2f(ex[j].y), bfhi(ex[j].y)};
      *(f32x4*)(a.out + (size_t)(64 * rt + 16 * j + (tid >> 4)) * D_ + 64 * ct + 4 * (tid & 15)) = xv + (2.f * sy + sk[j]) * gg[j];
    }
  }
  ST(14);
  ST_FLUSH(ws);
}

__global__ __launch_bounds__(512, 2) void k_fused(Args a) {
  extern __shared__ __attribute__((aligned(16))) char smem[];
  LAS char* L = (LAS char*)smem;
  const int w = __builtin_amdgcn_readfirstlane(threadIdx.x >> 6);
  v2u d0 = {0u, 0u};
  u64 pc; asm volatile("s_getpc_b64 %0" : "=s"(pc));
  if (w >= 1 && w < 4) {
    const unsigned o0 = (threadIdx.x - 64u) * 128u, o1 = o0 + 24576u < CODE_BYTES ? o0 + 24576u : 0u;
    asm volatile("global_load_dword %0, %2, %4\n\tglobal_load_dword %1, %3, %4" : "=&v"(d0.x), "=&v"(d0.y) : "v"(o0), "v"(o1), "s"(pc & ~127ull) : "memory");
  }
  if (w < 4) body<true>(a, L, w, d0);
  else body<false>(a, L, w, d0);
}
}

extern "C" void kernel_launch(void* const* d_in, const int* in_sizes, int n_in, void* d_out, int out_size, void* d_ws, size_t ws_size,
                              hipStream_t stream) {
  Args a;
  a.x = (const u16*)d_in[0]; a.Lre = (const u16*)d_in[1]; a.Lim = (const u16*)d_in[2];
  a.Bre = (const u16*)d_in[3]; a.Bim = (const u16*)d_in[4]; a.Cre = (const u16*)d_in[5]; a.Cim = (const u16*)d_in[6];
  a.logdt = (const u16*)d_in[7]; a.Dsk = (const u16*)d_in[8]; a.w1 = (const u16*)d_in[10]; a.w2 = (const u16*)d_in[11];
  a.ws = (char*)d_ws; a.out = (float*)d_out;
  (void)hipFuncSetAttribute((const void*)k_fused, hipFuncAttributeMaxDynamicSharedMemorySize, LDS_BYTES);
  k_fused<<<256, 512, LDS_BYTES, stream>>>(a);
}
```

```cpp
#include <hip/hip_runtime.h>
#include <hip/hip_bf16.h>
#include <stdint.h>
#include <utility>

typedef unsigned short u16;
typedef unsigned long long u64;
typedef unsigned v4u __attribute__((ext_vector_type(4)));
typedef unsigned v2u __attribute__((ext_vector_type(2)));
typedef float f32x4 __attribute__((ext_vector_type(4)));
typedef float f32x2 __attribute__((ext_vector_type(2)));
typedef short bf16x8 __attribute__((ext_vector_type(8)));
typedef short v4s __attribute__((ext_vector_type(4)));
typedef __bf16 v2bf __attribute__((ext_vector_type(2)));
#define LAS __attribute__((address_space(3)))
#define GAS __attribute__((address_space(1)))
#define RLX_AGENT __ATOMIC_RELAXED, __HIP_MEMORY_SCOPE_AGENT
#define RLX_WG __ATOMIC_RELAXED, __HIP_MEMORY_SCOPE_WORKGROUP

namespace {
constexpr int D_ = 512, F_ = 1792;
constexpr int KS2 = 88;
constexpr size_t WS_H = 0;
constexpr size_t WS_H_BYTES = (size_t)32 * KS2 * 4096;
constexpr size_t WS_ST = WS_H + WS_H_BYTES;
constexpr size_t WS_E = WS_ST + 2048 * 8;
constexpr size_t WS_FL = WS_E + 16 * 512 * 16;
constexpr size_t WS_EP = WS_FL + 16 * 256;
constexpr size_t WS_END = WS_EP + 128;
constexpr int SLOT = 32768, OFF_W = 16384;
constexpr int M_STT = 131072, M_CS = M_STT + 1024, M_PRM = M_CS + 512, M_ST2 = M_PRM + 512, M_CNT = M_ST2 + 512, M_TR = M_CNT + 256;
constexpr int LDS_BYTES = 163840;
constexpr unsigned CODE_BYTES = 49152;
constexpr unsigned SPIN_CAP = 1u << 17;
#define FAUX 16
constexpr int AUX_SC1 = 16;
#define RDM 0
#define GLM 4
#define WSLEEP 4
#define W2AT 1
#define PSLEEP 6

__device__ __forceinline__ float bf2f(unsigned b) { return __builtin_bit_cast(float, b << 16); }
__device__ __forceinline__ float bfhi(unsigned b) { return __builtin_bit_cast(float, b & 0xffff0000u); }
__device__ __forceinline__ unsigned pkbf(float lo, float hi) {
  v2bf v; v.x = (__bf16)lo; v.y = (__bf16)hi; return __builtin_bit_cast(unsigned, v);
}
__device__ __forceinline__ float rbf(float f) { return bf2f(pkbf(f, 0.f)); }
__device__ __forceinline__ f32x4 mfma(v4u a, v4u b, f32x4 c) {
  return __builtin_amdgcn_mfma_f32_16x16x32_bf16(__builtin_bit_cast(bf16x8, a), __builtin_bit_cast(bf16x8, b), c, 0, 0, 0);
}
__device__ __forceinline__ float dot2(unsigned a, unsigned b, float c) {
  return __builtin_amdgcn_fdot2_f32_bf16(__builtin_bit_cast(v2bf, a), __builtin_bit_cast(v2bf, b), c, false);
}
__device__ __forceinline__ void glds16(const void* g, LAS char* l) {
  __builtin_amdgcn_global_load_lds((const GAS void*)g, (LAS void*)l, 16, 0, 0);
}
__device__ __forceinline__ void touch(const void* g, LAS char* scratch) { __builtin_amdgcn_global_load_lds((const GAS void*)g, (LAS void*)scratch, 4, 0, 0); }
__device__ __forceinline__ v2u trread(LAS char* p) {
  return __builtin_bit_cast(v2u, __builtin_amdgcn_ds_read_tr16_b64_v4i16((LAS v4s*)p));
}
__device__ __forceinline__ unsigned silu2(unsigned hb) {
  const f32x2 h = {bf2f(hb), bfhi(hb)};
  const f32x2 t = h * -1.44269504088896f;
  const f32x2 d = f32x2{__builtin_amdgcn_exp2f(t.x), __builtin_amdgcn_exp2f(t.y)} + 1.f;
  const f32x2 y = h * f32x2{__builtin_amdgcn_rcpf(d.x), __builtin_amdgcn_rcpf(d.y)};
  return pkbf(y.x, y.y);
}
struct cplx { float r, i; };
__device__ __forceinline__ float fma_(float a, float b, float c) { float d; asm("v_fma_f32 %0, %1, %2, %3" : "=v"(d) : "v"(a), "v"(b), "v"(c)); return d; }
__device__ __forceinline__ float fnma_(float a, float b, float c) { float d; asm("v_fma_f32 %0, -%1, %2, %3" : "=v"(d) : "v"(a), "v"(b), "v"(c)); return d; }
__device__ __forceinline__ float mul_(float a, float b) { float d; asm("v_mul_f32 %0, %1, %2" : "=v"(d) : "v"(a), "v"(b)); return d; }
__device__ __forceinline__ float add_(float a, float b) { float d; asm("v_add_f32 %0, %1, %2" : "=v"(d) : "v"(a), "v"(b)); return d; }
__device__ __forceinline__ float mul_t(float a, float b) { float d; asm("s_nop 0\n\tv_mul_f32 %0, %1, %2" : "=v"(d) : "v"(a), "v"(b)); return d; }
__device__ __forceinline__ float add_t(float a, float b) { float d; asm("s_nop 0\n\tv_add_f32 %0, %1, %2" : "=v"(d) : "v"(a), "v"(b)); return d; }
__device__ __forceinline__ float silu_f(float h) { return mul_t(h, __builtin_amdgcn_rcpf(add_t(1.f, __builtin_amdgcn_exp2f(mul_(h, -1.44269504088896f))))); }
__device__ __forceinline__ cplx cmul(cplx a, cplx b) { return {fnma_(a.i, b.i, mul_(a.r, b.r)), fma_(a.r, b.i, mul_(a.i, b.r))}; }
__device__ __forceinline__ cplx cfma(cplx a, cplx b, cplx c) {
  return {fma_(a.r, b.r, fnma_(a.i, b.i, c.r)), fma_(a.r, b.i, fma_(a.i, b.r, c.i))};
}
__device__ __forceinline__ unsigned hashA(unsigned a, unsigned b) { unsigned h = a * 0x9E3779B1u + (b ^ (b >> 15)) * 0x85EBCA77u; return h ^ (h >> 13); }
__device__ __forceinline__ unsigned hashB(unsigned a, unsigned b) { unsigned h = b * 0xC2B2AE3Du + (a ^ (a >> 16)) * 0x27D4EB2Fu; return h ^ (h >> 15); }
template <int N> __device__ __forceinline__ void waitv() { asm volatile("s_waitcnt vmcnt(%0)" ::"n"(N) : "memory"); }
#define WAITL0() __builtin_amdgcn_s_waitcnt(0xC07F)
#define BAR() __builtin_amdgcn_s_barrier()
#define SB0() __builtin_amdgcn_sched_barrier(0)
#define CBAR() asm volatile("" ::: "memory")
#define ST_DECL
#define ST(i)
#define SF(i)
#define ST_FLUSH(wsp)

__device__ __forceinline__ unsigned lds_arrive(LAS unsigned* c) {
  unsigned o = 0;
  if ((threadIdx.x & 63) == 0)
    asm volatile("s_waitcnt lgkmcnt(0)\n\tds_add_rtn_u32 %0, %1, %2\n\ts_waitcnt lgkmcnt(0)" : "=&v"(o) : "v"((unsigned)(size_t)c), "v"(1u) : "memory");
  return __builtin_amdgcn_readfirstlane(o);
}
__device__ __forceinline__ void lds_wait(LAS unsigned* c, unsigned want) {
#pragma nounroll
  for (unsigned spins = 0; spins < (1u << 22); ++spins) {
    unsigned v;
    asm volatile("ds_read_b32 %0, %1\n\ts_waitcnt lgkmcnt(0)" : "=&v"(v) : "v"((unsigned)(size_t)c) : "memory");
    if (__builtin_amdgcn_readfirstlane(v) >= want) break;
    __builtin_amdgcn_s_sleep(1);
  }
}
struct Tr14 { v2u h[14]; };
template <int OFF> __device__ __forceinline__ void tr14(Tr14& t, const unsigned (&ad)[7]) {
  asm volatile("ds_read_b64_tr_b16 %0, %14 offset:%21\n\tds_read_b64_tr_b16 %1, %14 offset:%22\n\t"
               "ds_read_b64_tr_b16 %2, %15 offset:%21\n\tds_read_b64_tr_b16 %3, %15 offset:%22\n\t"
               "ds_read_b64_tr_b16 %4, %16 offset:%21\n\tds_read_b64_tr_b16 %5, %16 offset:%22\n\t"
               "ds_read_b64_tr_b16 %6, %17 offset:%21\n\tds_read_b64_tr_b16 %7, %17 offset:%22\n\t"
               "ds_read_b64_tr_b16 %8, %18 offset:%21\n\tds_read_b64_tr_b16 %9, %18 offset:%22\n\t"
               "ds_read_b64_tr_b16 %10, %19 offset:%21\n\tds_read_b64_tr_b16 %11, %19 offset:%22\n\t"
               "ds_read_b64_tr_b16 %12, %20 offset:%21\n\tds_read_b64_tr_b16 %13, %20 offset:%22"
               : "=&v"(t.h[0]), "=&v"(t.h[1]), "=&v"(t.h[2]), "=&v"(t.h[3]), "=&v"(t.h[4]), "=&v"(t.h[5]), "=&v"(t.h[6]),
                 "=&v"(t.h[7]), "=&v"(t.h[8]), "=&v"(t.h[9]), "=&v"(t.h[10]), "=&v"(t.h[11]), "=&v"(t.h[12]), "=&v"(t.h[13])
               : "v"(ad[0]), "v"(ad[1]), "v"(ad[2]), "v"(ad[3]), "v"(ad[4]), "v"(ad[5]), "v"(ad[6]), "n"(OFF), "n"(OFF + 1024)
               : "memory");
}
template <int OFF> __device__ __forceinline__ void rd2x(v4u (&x)[2], unsigned ad) {
  asm volatile("ds_read_b128 %0, %2 offset:%3\n\tds_read_b128 %1, %2 offset:%4" : "=&v"(x[0]), "=&v"(x[1]) : "v"(ad), "n"(OFF), "n"(OFF + 2048) : "memory");
}
template <int N> __device__ __forceinline__ void waitl_pin(Tr14& t, v4u (&x)[2]) {
  asm volatile("s_waitcnt lgkmcnt(%16)"
               : "+v"(t.h[0]), "+v"(t.h[1]), "+v"(t.h[2]), "+v"(t.h[3]), "+v"(t.h[4]), "+v"(t.h[5]), "+v"(t.h[6]), "+v"(t.h[7]), "+v"(t.h[8]),
                 "+v"(t.h[9]), "+v"(t.h[10]), "+v"(t.h[11]), "+v"(t.h[12]), "+v"(t.h[13]), "+v"(x[0]), "+v"(x[1])
               : "n"(N) : "memory");
}
template <int OFF> __device__ __forceinline__ void tr2(v2u& h0, v2u& h1, unsigned ad) {
  asm volatile("ds_read_b64_tr_b16 %0, %2 offset:%3\n\tds_read_b64_tr_b16 %1, %2 offset:%4" : "=&v"(h0), "=&v"(h1) : "v"(ad), "n"(OFF), "n"(OFF + 1024) : "memory");
}
__device__ __forceinline__ void keep(const Tr14& t, const v4u (&x)[2]) {
  asm volatile("" :: "v"(t.h[0]), "v"(t.h[1]), "v"(t.h[2]), "v"(t.h[3]), "v"(t.h[4]), "v"(t.h[5]), "v"(t.h[6]), "v"(t.h[7]), "v"(t.h[8]),
               "v"(t.h[9]), "v"(t.h[10]), "v"(t.h[11]), "v"(t.h[12]), "v"(t.h[13]), "v"(x[0]), "v"(x[1]));
}
__device__ __forceinline__ void waitl_pinS(v4u (&x)[2], v2u (&t)[4]) {
  asm volatile("s_waitcnt lgkmcnt(0)" : "+v"(x[0]), "+v"(x[1]), "+v"(t[0]), "+v"(t[1]), "+v"(t[2]), "+v"(t[3]) :: "memory");
}
template <int OFF> __device__ __forceinline__ void tr4(v2u (&h)[4], unsigned a0, unsigned a1) {
  asm volatile("ds_read_b64_tr_b16 %0, %4 offset:%6\n\tds_read_b64_tr_b16 %1, %4 offset:%7\n\t"
               "ds_read_b64_tr_b16 %2, %5 offset:%6\n\tds_read_b64_tr_b16 %3, %5 offset:%7"
               : "=&v"(h[0]), "=&v"(h[1]), "=&v"(h[2]), "=&v"(h[3]) : "v"(a0), "v"(a1), "n"(OFF), "n"(OFF + 1024) : "memory");
}
template <int OFF> __device__ __forceinline__ void rd4w(v4u (&x)[4], unsigned ad) {
  asm volatile("ds_read_b128 %0, %4 offset:%5\n\tds_read_b128 %1, %4 offset:%6\n\tds_read_b128 %2, %4 offset:%7\n\tds_read_b128 %3, %4 offset:%8"
               : "=&v"(x[0]), "=&v"(x[1]), "=&v"(x[2]), "=&v"(x[3]) : "v"(ad), "n"(OFF), "n"(OFF + 2048), "n"(OFF + 4096), "n"(OFF + 6144) : "memory");
}
template <int OFF> __device__ __forceinline__ void rd1(v4u& x, unsigned ad) {
  asm volatile("ds_read_b128 %0, %1 offset:%2" : "=&v"(x) : "v"(ad), "n"(OFF) : "memory");
}
template <int N> __device__ __forceinline__ void waitl_pin5(v4u (&x)[4], v4u& y) {
  asm volatile("s_waitcnt lgkmcnt(%5)" : "+v"(x[0]), "+v"(x[1]), "+v"(x[2]), "+v"(x[3]), "+v"(y) : "n"(N) : "memory");
}
template <int N> __device__ __forceinline__ void waitl_pin4(v4u (&x)[4]) {
  asm volatile("s_waitcnt lgkmcnt(%4)" : "+v"(x[0]), "+v"(x[1]), "+v"(x[2]), "+v"(x[3]) : "n"(N) : "memory");
}
__device__ __forceinline__ float fdiv(float a, float b) {
  const float rc = __builtin_amdgcn_rcpf(b), q0 = mul_t(a, rc);
  return fma_(fnma_(q0, b, a), rc, q0);
}
typedef std::integral_constant<int, 0> I0;
#define IC(n) std::integral_constant<int, (n)>{}
#define CAS __attribute__((address_space(4)))
__device__ __forceinline__ void wload(unsigned& d, const char* p) { asm volatile("global_load_dword %0, %1, off" : "=&v"(d) : "v"(p) : "memory"); }
struct W8 { v2u h[8]; };
__device__ __forceinline__ void tr2k(v2u& h0, v2u& h1, unsigned ad) {
  asm volatile("ds_read_b64_tr_b16 %0, %2\n\tds_read_b64_tr_b16 %1, %2 offset:2048" : "=&v"(h0), "=&v"(h1) : "v"(ad) : "memory");
}
__device__ __forceinline__ void rd2h(v2u& h0, v2u& h1, unsigned ad) {
  asm volatile("ds_read_b64 %0, %2\n\tds_read_b64 %1, %2 offset:8" : "=&v"(h0), "=&v"(h1) : "v"(ad) : "memory");
}
__device__ __forceinline__ void waitl_pin8(W8& t) {
  asm volatile("s_waitcnt lgkmcnt(0)" : "+v"(t.h[0]), "+v"(t.h[1]), "+v"(t.h[2]), "+v"(t.h[3]), "+v"(t.h[4]), "+v"(t.h[5]), "+v"(t.h[6]), "+v"(t.h[7]) :: "memory");
}
__device__ __forceinline__ void keep8(const W8& t) {
  asm volatile("" :: "v"(t.h[0]), "v"(t.h[1]), "v"(t.h[2]), "v"(t.h[3]), "v"(t.h[4]), "v"(t.h[5]), "v"(t.h[6]), "v"(t.h[7]));
}
__device__ __forceinline__ void pin4(v4u (&x)[4]) { asm volatile("" : "+v"(x[0]), "+v"(x[1]), "+v"(x[2]), "+v"(x[3])); }
template <int OFF> __device__ __forceinline__ void aload1(v4u& r, unsigned voff, const char* sbase) {
  asm volatile("global_load_dwordx4 %0, %1, %2 offset:%3 sc1" : "=&v"(r) : "v"(voff), "s"(sbase), "n"(OFF) : "memory");
}
__device__ __forceinline__ void poll_flags(const char* p, int n, u64 tag) {
  const int lane = threadIdx.x & 63;
  GAS u64* f = (GAS u64*)p + (lane < n ? lane : 0);
#pragma nounroll
  for (unsigned spins = 0; spins < SPIN_CAP; ++spins) {
    if (__all(__hip_atomic_load(f, RLX_AGENT) == tag)) break;
    __builtin_amdgcn_s_sleep(PSLEEP);
  }
  __builtin_amdgcn_fence(__ATOMIC_ACQUIRE, "wavefront");
}
__device__ __forceinline__ void aload4(v4u (&r)[4], unsigned voff, const char* sbase) {
  asm volatile("global_load_dwordx4 %0, %4, %5 sc1\n\tglobal_load_dwordx4 %1, %4, %5 offset:1024 sc1\n\t"
               "global_load_dwordx4 %2, %4, %5 offset:2048 sc1\n\tglobal_load_dwordx4 %3, %4, %5 offset:3072 sc1"
               : "=&v"(r[0]), "=&v"(r[1]), "=&v"(r[2]), "=&v"(r[3]) : "v"(voff), "s"(sbase) : "memory");
}
__device__ __forceinline__ void trread8(v4u (&wf)[4], unsigned a0, unsigned a1, unsigned a2, unsigned a3) {
  v2u x0, x1, x2, x3, x4, x5, x6, x7;
  asm volatile("ds_read_b64_tr_b16 %0, %8\n\tds_read_b64_tr_b16 %1, %8 offset:2048\n\t"
               "ds_read_b64_tr_b16 %2, %9\n\tds_read_b64_tr_b16 %3, %9 offset:2048\n\t"
               "ds_read_b64_tr_b16 %4, %10\n\tds_read_b64_tr_b16 %5, %10 offset:2048\n\t"
               "ds_read_b64_tr_b16 %6, %11\n\tds_read_b64_tr_b16 %7, %11 offset:2048\n\ts_waitcnt lgkmcnt(0)"
               : "=&v"(x0), "=&v"(x1), "=&v"(x2), "=&v"(x3), "=&v"(x4), "=&v"(x5), "=&v"(x6), "=&v"(x7)
               : "v"(a0), "v"(a1), "v"(a2), "v"(a3) : "memory");
  wf[0] = v4u{x0.x, x0.y, x1.x, x1.y}; wf[1] = v4u{x2.x, x2.y, x3.x, x3.y};
  wf[2] = v4u{x4.x, x4.y, x5.x, x5.y}; wf[3] = v4u{x6.x, x6.y, x7.x, x7.y};
}
constexpr int p2_wait(int i, int N, int W, int RD) {
  int total = 0, need = 0;
  for (int j = 0; j < W && j < N; j++) { total += 4; if (j == i) need = total; }
  for (int s = 0; s < i; s++) {
    if (s + RD < N) { total += 4; if (s + RD == i + 1) need = total; }
    if (s + W < N) { total += 4; if (s + W == i) need = total > need ? total : need; }
  }
  return total - need;
}

template <int... I, class Fn> __device__ __forceinline__ void static_for_(std::integer_sequence<int, I...>, Fn&& f) { (f(std::integral_constant<int, I>{}), ...); }
template <int N, class Fn> __device__ __forceinline__ void static_for(Fn&& f) { static_for_(std::make_integer_sequence<int, N>{}, f); }

struct Args {
  const u16 *x, *Lre, *Lim, *Bre, *Bim, *Cre, *Cim, *logdt, *Dsk, *w1, *w2;
  char* ws; float* out;
};

template <bool FFN>
__device__ __forceinline__ void body(const Args& a, LAS char* L, const int w) {
  const int b = blockIdx.x, xcd = b & 7, idx = b >> 3;
  const int r = (xcd & 1) * 8 + (idx & 7), c = (xcd >> 1) * 4 + (idx >> 3);
  const int rt = 4 * xcd + (idx & 3), ct = idx >> 2;
  const int tid = threadIdx.x, lane = tid & 63, tok = lane & 15, g = lane >> 4, tg = w & 3;
  const int codd = c & 1;
  char* const ws = a.ws;
  LAS unsigned* CNT = (LAS unsigned*)(L + M_CNT);
  ST_DECL ST(0);
  const __amdgpu_buffer_rsrc_t rsw = __builtin_amdgcn_make_buffer_rsrc((void*)ws, 0, (int)WS_END, 0x00020000);
  const int r8 = lane >> 3, p8 = lane & 7;
  constexpr int N2 = FFN ? 14 : 8;
  const int s0 = FFN ? 14 * tg : 56 + 8 * tg;
  const char* bw0 = (const char*)(a.w2 + 64 * ct);
  const unsigned bwl = (r8 * D_ + (((((p8 >> 1) ^ ((r8 >> 1) & 3)) << 1) | (p8 & 1)) << 3)) * 2;
  const int r4 = lane >> 2, c4 = lane & 3;
  const size_t bc0 = (size_t)(64 * ct) * 1024;
  const unsigned bcl = (r4 * 512 + ((c4 ^ ((-(r4 >> 2)) & 3)) << 3)) * 2;
  constexpr int RD = FFN ? 3 : 5, WA = FFN ? 4 : 7;
  auto ringp = [&](int i) {
    const int e = i % RD;
    return L + (FFN ? 65536 + tg * 12288 + e * 4096 : e == 0 ? 114688 + tg * 4096 : e < 3 ? 32768 + tg * 8192 + (e - 1) * 4096 : tg * 8192 + (e - 3) * 4096);
  };
  auto srcF = [&](int f, int i, int j) { return bw0 + (size_t)(32 * (14 * f + i) + 8 * j) * (D_ * 2) + bwl; };
  auto srcS = [&](int f, int i, int j) { return (const char*)((i & 1) ? a.Cim : a.Cre) + bc0 + 256 * f + 64 * (i >> 1) + (size_t)j * 16 * 1024 + bcl; };
  auto issueB = [&](int i) {
    LAS char* sl = ringp(i);
#pragma unroll
    for (int j = 0; j < 4; j++) glds16(FFN ? srcF(tg, i, j) : srcS(tg, i, j), sl + j * 1024);
  };
  const int swz = (tok >> 1) & 7;
  const int xoff = (32 * tg + tok) * 128 + ((g ^ swz) << 4);
  const int q = tok >> 2, p4 = lane & 3, fl = q | ((g & 1) << 2);
  const unsigned lb = (unsigned)(size_t)L;
  const unsigned xad[4] = {lb + xoff, lb + (xoff ^ 64), lb + xoff + 2 * SLOT, lb + (xoff ^ 64) + 2 * SLOT};
  auto waddr = [&](int v) { const int pi = codd ? (v == 6 ? 1 : v + 2) : v; return lb + OFF_W + (8 * g + q) * 256 + ((pi ^ fl) << 5) + p4 * 8; };
  constexpr unsigned ONES = 0x3f803f80u;
  LAS f32x2* STT = (LAS f32x2*)(L + M_STT);
  LAS float* CS = (LAS float*)(L + M_CS);
  u64 tag;

  if (FFN) {
    tag = *(const CAS u64*)(ws + WS_EP) + 1ull;
    if (tid < 16) CNT[tid] = 0u;
    LAS char* junk = L + M_TR + 16896 + tg * 256;
    if (w == 0 && lane < 32) {
      const float lr = bf2f(a.Lre[32 * c + lane]), li = bf2f(a.Lim[32 * c + lane]), dt = rbf(expf(bf2f(a.logdt[32 * c + lane])));
      const float er = expf(dt * lr); float sn, cn; sincosf(dt * li, &sn, &cn);
      const float br = er * cn, bi = er * sn, den = 1.f / (lr * lr + li * li), nr = br - 1.f;
      *(LAS f32x4*)(L + M_PRM + lane * 16) = f32x4{br, bi, (nr * lr + bi * li) * den, (bi * lr - nr * li) * den};
    }
    if (tid < 152) {
      const int t = b * 152 + tid;
      const char* p = t < 14336 ? (const char*)a.w1 + (size_t)t * 128
                    : t < 30720 ? (const char*)a.x + (size_t)(t - 14336) * 128
                    : t < 34816 ? (const char*)a.Bre + (size_t)(t - 30720) * 128 : (const char*)a.Bim + (size_t)(t - 34816) * 128;
      __builtin_amdgcn_s_sleep(WSLEEP);
      touch(p, junk);
    }
    const int t2 = b * 88 + (tid < 88 ? tid : 0);
    const char* wp2 = t2 < 14336 ? (const char*)a.w2 + (size_t)t2 * 128
                    : t2 < 18432 ? (const char*)a.Cre + (size_t)(t2 - 14336) * 128 : (const char*)a.Cim + (size_t)(t2 - 18432) * 128;
    unsigned wlo[7], whi[7];
#pragma unroll
    for (int v = 0; v < 7; v++) { wlo[v] = waddr(v); whi[v] = wlo[v] + 2 * SLOT; }
    f32x4 acc[7][2];
#pragma unroll
    for (int u = 0; u < 7; u++) { acc[u][0] = f32x4{0, 0, 0, 0}; acc[u][1] = f32x4{0, 0, 0, 0}; }
    v4u xf[2][2]; Tr14 wt[2];
    auto rd = [&](auto kc) {
      constexpr int ks = decltype(kc)::value, kk = ks & 1, sl = (ks >> 1) & 3;
      rd2x<(sl & 1) * SLOT>(xf[kk], xad[(sl >> 1) * 2 + kk]);
      tr14<(sl & 1) * SLOT + kk * 8192>(wt[kk], (sl >> 1) ? whi : wlo);
    };
    auto step = [&](auto kc) {
      constexpr int ks = decltype(kc)::value, kk = ks & 1, kn = ks + 1, nk = kn & 1, sl = (kn >> 1) & 3;
      static_for<14>([&](auto mc) {
        constexpr int m = decltype(mc)::value, v = m >> 1, tt = m & 1;
        const v4u wv = {wt[kk].h[2 * v].x, wt[kk].h[2 * v].y, wt[kk].h[2 * v + 1].x, wt[kk].h[2 * v + 1].y};
        acc[v][tt] = mfma(wv, xf[kk][tt], acc[v][tt]);
        SB0();
        if constexpr (kn < 16) {
          if (m == 0) rd2x<(sl & 1) * SLOT>(xf[nk], xad[(sl >> 1) * 2 + nk]);
          if constexpr (m >= 1 && m <= 7) tr2<(sl & 1) * SLOT + nk * 8192>(wt[nk].h[2 * (m - 1)], wt[nk].h[2 * (m - 1) + 1], (sl >> 1) ? whi[m - 1] : wlo[m - 1]);
          SB0();
        }
      });
      keep(wt[kk], xf[kk]);
      if constexpr (kn < 16) waitl_pin<0>(wt[nk], xf[nk]);
    };
    WAITL0(); BAR();
    ST(1);
    rd(IC(0));
    waitl_pin<0>(wt[0], xf[0]);
    static_for<8>([&](auto sc) {
      constexpr int s = decltype(sc)::value;
      SF(1);
      step(IC(2 * s));
      SF(5);
      BAR();
      SF(6);
      if (s == 3) ST(2);
      if (s == W2AT && tg < 2) touch(wp2, junk);
      step(IC(2 * s + 1));
      SF(8);
    });
    ST(3);
    BAR();
    ST(4);
    {
      __builtin_amdgcn_s_setprio(3);
      const f32x2 st0 = STT[32 * tg + tok], st1 = STT[32 * tg + 16 + tok];
      f32x4 cs[7];
#pragma unroll
      for (int v = 0; v < 7; v++) cs[v] = *(LAS f32x4*)(CS + v * 16 + 4 * g);
      const int T16 = 8 * r + 2 * tg;
      const int fb = (int)WS_H + ((T16 >> 2) * KS2 * 4 + (T16 & 3)) * 1024 + lane * 16;
      const float km[2] = {-st0.x * st0.y, -st1.x * st1.y}, rs[2] = {st0.y, st1.y};
      v2u o[2][2];
#pragma unroll
      for (int v = 0; v < 7; v++) {
#pragma unroll
        for (int tt = 0; tt < 2; tt++) {
          unsigned pk[2];
#pragma unroll
          for (int hh = 0; hh < 2; hh++) {
            const f32x2 ac = {acc[v][tt][2 * hh], acc[v][tt][2 * hh + 1]}, cc = {cs[v][2 * hh], cs[v][2 * hh + 1]};
            const f32x2 u = __builtin_elementwise_fma(ac, f32x2{rs[tt], rs[tt]}, cc * km[tt]);
            unsigned hb = pkbf(u.x, u.y);
            asm volatile("" : "+v"(hb));
            pk[hh] = silu2(hb);
          }
          o[v & 1][tt] = v2u{pk[0], pk[1]};
        }
        if (v & 1) {
          const int U = 7 * c + v - 1 + codd;
#pragma unroll
          for (int tt = 0; tt < 2; tt++)
            __builtin_amdgcn_raw_buffer_store_b128(v4u{o[0][tt].x, o[0][tt].y, o[1][tt].x, o[1][tt].y}, rsw, fb + tt * 1024 + (U >> 1) * 4096, 0, FAUX);
        } else if (v == 6) {
          const int U = codd ? 7 * c : 7 * c + 6;
#pragma unroll
          for (int tt = 0; tt < 2; tt++)
            __builtin_amdgcn_raw_buffer_store_b64(o[0][tt], rsw, fb + tt * 1024 + (U >> 1) * 4096 + (U & 1) * 8, 0, FAUX);
        }
      }
    }
    ST(5);
    lds_arrive(CNT + 7);
    waitv<0>();
    if (lds_arrive(CNT + 3) == 3u) {
      lds_wait(CNT + 0, 4u);
      if (lane == 0) __hip_atomic_store((GAS u64*)(ws + WS_FL + r * 256 + c * 8), tag, RLX_AGENT);
    }
    ST(6);
    __builtin_amdgcn_s_setprio(0);
  } else {
    unsigned sx[2], sw[2];
#pragma unroll
    for (int h = 0; h < 2; h++) {
      sx[h] = ((8 * h + r8) * D_ + ((p8 ^ ((4 * h + (r8 >> 1)) & 7)) << 3)) * 2;
      sw[h] = ((8 * h + g) * F_ + (((((tok >> 1) ^ (g | (h << 2))) << 1) | (tok & 1)) << 3)) * 2;
    }
    const char* xb = (const char*)(a.x + (size_t)(128 * r + 32 * tg) * D_);
    const char* wb1 = (const char*)(a.w1 + (size_t)(16 * tg) * F_ + 112 * c - 16 * codd);
    const int rot = idx & 7;
    auto issue1 = [&](int s, int j) {
      LAS char* sb = L + (s & 3) * SLOT;
      const int k0 = ((s + rot) & 7) * 64;
      if (j < 4) glds16(xb + ((j >> 1) * 16 * D_ + k0) * 2 + sx[j & 1], sb + (4 * tg + j) * 1024);
      else glds16(wb1 + (size_t)(k0 + (j & 1) * 4) * F_ * 2 + sw[(j - 4) >> 1], sb + OFF_W + (4 * tg + j - 4) * 1024);
    };
    auto issue = [&](int s) {
#pragma unroll
      for (int j = 0; j < 8; j++) issue1(s, j);
    };
    issue(0); issue(1);
    __builtin_amdgcn_s_setprio(3);
    tag = *(const CAS u64*)(ws + WS_EP) + 1ull;
    const u16* ssp[2];
#pragma unroll
    for (int h = 0; h < 2; h++) {
      const int n = 16 * tg + 8 * h + r8;
      ssp[h] = ((n & 1) ? a.Bim : a.Bre) + (size_t)(32 * c + (n >> 1)) * D_ + ((p8 ^ ((n >> 1) & 7)) << 3);
    }
    auto issueS1 = [&](int js, int j) { glds16(ssp[j & 1] + ((4 * js + (j >> 1) + rot) & 7) * 64, L + js * SLOT + (j >> 1) * 8192 + (2 * tg + (j & 1)) * 1024); };
    auto issueR1 = [&](int i, int j) {
      const int o = 32 * i + 8 * tg + j;
      if (o < 48) glds16(srcF(o / 12, (o % 12) >> 2, o & 3), L + 65536 + o * 1024);
      else glds16(srcS((o - 48) >> 2, 0, o & 3), L + 65536 + o * 1024);
    };
    auto piece = [&](int s, int j) {
      if (s <= 3) issue1(s + 4, j); else if (s <= 5) issueS1(s - 4, j); else issueR1(s - 6, j);
    };
    const unsigned wa0 = waddr(tg), wa1 = waddr(tg == 3 ? 3 : tg + 4);
    const v4u ones = {ONES, ONES, ONES, ONES};
    v4u xr[16][2];
    v2u wq[2][4];
    f32x4 sm[2] = {{0, 0, 0, 0}, {0, 0, 0, 0}}, sq[2] = {{0, 0, 0, 0}, {0, 0, 0, 0}}, cf[2] = {{0, 0, 0, 0}, {0, 0, 0, 0}};
    auto rdS = [&](auto kc) {
      constexpr int ks = decltype(kc)::value, kk = ks & 1, sl = (ks >> 1) & 3;
      rd2x<(sl & 1) * SLOT>(xr[ks], xad[(sl >> 1) * 2 + kk]);
      tr4<(sl & 1) * SLOT + kk * 8192>(wq[kk], wa0 + (sl >> 1) * 2 * SLOT, wa1 + (sl >> 1) * 2 * SLOT);
    };
    auto pinS = [&](auto kc) {
      constexpr int ks = decltype(kc)::value, kk = ks & 1;
      waitl_pinS(xr[ks], wq[kk]);
    };
    auto stat = [&](auto kc, int s, int j0) {
      constexpr int ks = decltype(kc)::value, kk = ks & 1;
      sm[0] = mfma(ones, xr[ks][0], sm[0]); sq[0] = mfma(xr[ks][0], xr[ks][0], sq[0]);
      SB0(); if (j0 >= 0) piece(s, j0); SB0();
      sm[1] = mfma(ones, xr[ks][1], sm[1]);
      SB0(); if (j0 >= 0) piece(s, j0 + 1); SB0();
      sq[1] = mfma(xr[ks][1], xr[ks][1], sq[1]);
      SB0(); if (j0 >= 0) piece(s, j0 + 2); SB0();
      cf[0] = mfma(v4u{wq[kk][0].x, wq[kk][0].y, wq[kk][1].x, wq[kk][1].y}, ones, cf[0]);
      SB0(); if (j0 >= 0) piece(s, j0 + 3); SB0();
      cf[1] = mfma(v4u{wq[kk][2].x, wq[kk][2].y, wq[kk][3].x, wq[kk][3].y}, ones, cf[1]);
      SB0();
    };
    const int sch = lane & 7, seg = lane >> 3, chg = 32 * c + 8 * tg + sch;
    cplx lam, coef, l16, l32, l64, lseg = {1.f, 0.f}, wa = {0.f, 0.f}, wb = {0.f, 0.f};
    waitv<8>();
    BAR();
    ST(1);
    rdS(IC(0));
    issue(2);
    pinS(IC(0));
    static_for<8>([&](auto sc) {
      constexpr int s = decltype(sc)::value;
      SF(1);
      rdS(IC(2 * s + 1));
      if (s == 0) { stat(IC(0), -1, 0); issue1(3, 4); issue1(3, 5); issue1(3, 6); issue1(3, 7); }
      else stat(IC(2 * s), s - 1, 4);
      pinS(IC(2 * s + 1));
      SF(2);
      if (s <= 6) waitv<16>();
      SF(3);
      BAR();
      SF(6);
      if (s == 3) ST(2);
      if constexpr (s < 7) rdS(IC(2 * s + 2));
      stat(IC(2 * s + 1), s, 0);
      if constexpr (s < 7) pinS(IC(2 * s + 2));
      SF(8);
      if (s == 1) {
        const f32x4 pr = *(LAS f32x4*)(L + M_PRM + (8 * tg + sch) * 16);
        lam = {pr[0], pr[1]}; coef = {pr[2], pr[3]};
        const cplx l2 = cmul(lam, lam), l4 = cmul(l2, l2), l8 = cmul(l4, l4);
        l16 = cmul(l8, l8); l32 = cmul(l16, l16); l64 = cmul(l32, l32);
        const cplx l128 = cmul(l64, l64), l256 = cmul(l128, l128), l512 = cmul(l256, l256), l1024 = cmul(l512, l512);
        if (seg & 1) lseg = l16;
        if (seg & 2) lseg = cmul(lseg, l32);
        if (seg & 4) lseg = cmul(lseg, l64);
        const int nb = r - 1 - (seg + 8);
        cplx t = {1.f, 0.f};
        const int ne = nb >= 0 ? nb : (nb + 8 >= 0 ? nb + 8 : 0);
        if (ne & 1) t = l128;
        if (ne & 2) t = cmul(t, l256);
        if (ne & 4) t = cmul(t, l512);
        if (nb >= 0) { wb = t; wa = cmul(t, l1024); } else if (nb + 8 >= 0) { wa = t; }
        asm volatile("" : "+v"(wa.r), "+v"(wa.i), "+v"(wb.r), "+v"(wb.i), "+v"(lseg.r), "+v"(lseg.i));
      }
    });
    {
      const int e = tok & 3;
      const float d0 = e == 0 ? sq[0][0] : e == 1 ? sq[0][1] : e == 2 ? sq[0][2] : sq[0][3];
      const float d1 = e == 0 ? sq[1][0] : e == 1 ? sq[1][1] : e == 2 ? sq[1][2] : sq[1][3];
      const bool own = g == q;
#pragma unroll
      for (int tt = 0; tt < 2; tt++) {
        float m = sm[tt][0] * (1.f / 512.f);
        asm volatile("" : "+v"(m));
        const float var = fmaxf(fnma_(m, m, mul_(tt ? d1 : d0, 1.f / 512.f)), 0.f);
        const float mb = rbf(m), sd = rbf(__builtin_amdgcn_sqrtf(rbf(rbf(var) + 1e-5f)));
        if (own) {
          STT[32 * tg + 16 * tt + tok] = f32x2{mb, fdiv(1.f, sd)};
          if (c == 0)
            __builtin_amdgcn_raw_buffer_store_b64(v2u{__builtin_bit_cast(unsigned, mb), __builtin_bit_cast(unsigned, sd)}, rsw,
                                                  (int)WS_ST + (128 * r + 32 * tg + 16 * tt + tok) * 8, 0, AUX_SC1);
        }
      }
      if (tok == 0) {
        *(LAS f32x4*)(CS + tg * 16 + 4 * g) = cf[0];
        if (tg < 3) *(LAS f32x4*)(CS + (tg + 4) * 16 + 4 * g) = cf[1];
      }
    }
    ST(3);
    WAITL0(); BAR();
    ST(4);
#pragma unroll
    for (int j = 4; j < 8; j++) piece(7, j);
    __builtin_amdgcn_s_setprio(0);
    f32x4 acc[4][2];
    f32x4 cs4 = {0, 0, 0, 0};
#pragma unroll
    for (int u = 0; u < 4; u++) { acc[u][0] = f32x4{0, 0, 0, 0}; acc[u][1] = f32x4{0, 0, 0, 0}; }
    const unsigned sad[2] = {lb + tok * 128 + ((g ^ swz) << 4), lb + ((tok * 128 + ((g ^ swz) << 4)) ^ 64)};
    v4u wf[3][4], wo[3];
    const unsigned oad[2] = {sad[0] + tg * 2048, sad[1] + tg * 2048};
    waitv<0>();
    lds_arrive(CNT + 0); lds_wait(CNT + 0, 4u);
    rd4w<0>(wf[0], sad[0]); rd1<0>(wo[0], oad[0]);
    rd4w<0>(wf[1], sad[1]); rd1<0>(wo[1], oad[1]);
    lds_wait(CNT + 7, 4u);
    static_for<16>([&](auto kc) {
      constexpr int ks = decltype(kc)::value, kn = ks + 2;
      if constexpr (kn < 16) {
        rd4w<(kn >> 3) * SLOT + ((kn >> 1) & 3) * 8192>(wf[kn % 3], sad[kn & 1]);
        rd1<(kn >> 3) * SLOT + ((kn >> 1) & 3) * 8192>(wo[kn % 3], oad[kn & 1]);
        waitl_pin5<10>(wf[ks % 3], wo[ks % 3]);
      } else if constexpr (kn == 16) waitl_pin5<5>(wf[ks % 3], wo[ks % 3]);
      else waitl_pin5<0>(wf[ks % 3], wo[ks % 3]);
      SB0();
#pragma unroll
      for (int u = 0; u < 4; u++) { acc[u][0] = mfma(wf[ks % 3][u], xr[ks][0], acc[u][0]); acc[u][1] = mfma(wf[ks % 3][u], xr[ks][1], acc[u][1]); }
      cs4 = mfma(wo[ks % 3], ones, cs4);
      if (ks == 8) lds_arrive(CNT + 1);
      SB0();
    });
    ST(5);
    __builtin_amdgcn_s_setprio(3);
    lds_wait(CNT + 1, 4u);
#pragma unroll
    for (int u = 0; u < 4; u++)
#pragma unroll
      for (int tt = 0; tt < 2; tt++) {
        const int token = 32 * tg + 16 * tt + tok;
        *(LAS f32x4*)(L + token * 256 + (((4 * u + g) ^ (token & 7) ^ (((token >> 4) & 3) << 2)) << 4)) = acc[u][tt];
      }
    const float ca = lane & 1 ? cs4[2] : cs4[0], cb = lane & 1 ? cs4[3] : cs4[1];
    const f32x2 c2 = {__shfl(ca, (sch >> 1) * 16 + (sch & 1)), __shfl(cb, (sch >> 1) * 16 + (sch & 1))};
    lds_arrive(CNT + 2);
    lds_wait(CNT + 2, 4u);
    cplx bu[16], h = {0.f, 0.f}, init0 = {0.f, 0.f};
    asm volatile("" : "+v"(l16.r), "+v"(l16.i));
    l32 = cmul(l16, l16); l64 = cmul(l32, l32);
    {
      const int rbase = seg * 4096 + (sch & 1) * 8, rch = (4 * tg + (sch >> 1)) ^ ((seg & 3) << 2);
      f32x2 v[16]; f32x4 st[8];
#pragma unroll
      for (int t = 0; t < 16; t++) v[t] = *(LAS f32x2*)(L + rbase + t * 256 + ((rch ^ (t & 7)) << 4));
#pragma unroll
      for (int t = 0; t < 8; t++) st[t] = *(LAS f32x4*)(L + M_STT + (16 * seg + 2 * t) * 8);
#pragma unroll
      for (int t = 0; t < 16; t++) {
        const float mu = st[t >> 1][(t & 1) * 2], rs = st[t >> 1][(t & 1) * 2 + 1];
        bu[t] = {mul_(fnma_(mu, c2.x, v[t].x), rs), mul_(fnma_(mu, c2.y, v[t].y), rs)};
        h = cfma(lam, h, bu[t]);
      }
      { cplx o = {__shfl_up(h.r, 8), __shfl_up(h.i, 8)}; if (seg >= 1) h = cfma(l16, o, h); }
      { cplx o = {__shfl_up(h.r, 16), __shfl_up(h.i, 16)}; if (seg >= 2) h = cfma(l32, o, h); }
      { cplx o = {__shfl_up(h.r, 32), __shfl_up(h.i, 32)}; if (seg >= 4) h = cfma(l64, o, h); }
      if (seg == 7 && r < 15) {
        const unsigned ur = __builtin_bit_cast(unsigned, h.r), ui = __builtin_bit_cast(unsigned, h.i);
        GAS u64* gp = (GAS u64*)(ws + WS_E + ((size_t)r * 512 + chg) * 16);
        __hip_atomic_store(gp, ((u64)((unsigned)tag + hashA(ur, ui)) << 32) | ur, RLX_AGENT);
        __hip_atomic_store(gp + 1, ((u64)((unsigned)(tag >> 32) + hashB(ur, ui)) << 32) | ui, RLX_AGENT);
      }
      { cplx o = {__shfl_up(h.r, 8), __shfl_up(h.i, 8)}; if (seg >= 1) init0 = o; }
    }
    ST(6);
    lds_arrive(CNT + 5);
    issueB(1); issueB(2);
    cplx carry = {0.f, 0.f};
    if (r > 0) {
      const bool needA = seg < r, needB = seg + 8 < r;
      GAS u64* ga = (GAS u64*)(ws + WS_E + ((size_t)seg * 512 + chg) * 16);
      GAS u64* gb = ga + (size_t)8 * 512 * 2;
      u64 a0 = 0, a1 = 0, b0 = 0, b1 = 0;
#pragma nounroll
      for (unsigned spins = 0; spins < SPIN_CAP; ++spins) {
        bool ok = true;
        if (needA) { a0 = __hip_atomic_load(ga, RLX_AGENT); a1 = __hip_atomic_load(ga + 1, RLX_AGENT); }
        if (needB) { b0 = __hip_atomic_load(gb, RLX_AGENT); b1 = __hip_atomic_load(gb + 1, RLX_AGENT); }
        if (needA) ok = (unsigned)(a0 >> 32) == (unsigned)tag + hashA((unsigned)a0, (unsigned)a1) &&
                        (unsigned)(a1 >> 32) == (unsigned)(tag >> 32) + hashB((unsigned)a0, (unsigned)a1);
        if (needB) ok = ok && (unsigned)(b0 >> 32) == (unsigned)tag + hashA((unsigned)b0, (unsigned)b1) &&
                        (unsigned)(b1 >> 32) == (unsigned)(tag >> 32) + hashB((unsigned)b0, (unsigned)b1);
        if (__all(ok)) break;
        __builtin_amdgcn_s_sleep(2);
      }
      const cplx ea = {__builtin_bit_cast(float, (unsigned)a0), __builtin_bit_cast(float, (unsigned)a1)};
      const cplx eb = {__builtin_bit_cast(float, (unsigned)b0), __builtin_bit_cast(float, (unsigned)b1)};
      cplx s1 = {0.f, 0.f};
      if (needA) s1 = cmul(wa, ea);
      if (needB) s1 = cfma(wb, eb, s1);
      s1.r = add_(s1.r, __shfl_xor(s1.r, 8)); s1.i = add_(s1.i, __shfl_xor(s1.i, 8));
      s1.r = add_(s1.r, __shfl_xor(s1.r, 16)); s1.i = add_(s1.i, __shfl_xor(s1.i, 16));
      s1.r = add_(s1.r, __shfl_xor(s1.r, 32)); s1.i = add_(s1.i, __shfl_xor(s1.i, 32));
      carry = s1;
    }
    ST(7);
    h = cfma(lseg, carry, init0);
    LAS char* tr = L + M_TR + tg * 4224;
#pragma unroll
    for (int t = 0; t < 16; t++) {
      h = cfma(lam, h, bu[t]);
      const cplx o = cmul(coef, h);
      const unsigned pk = pkbf(o.r, -o.i);
      LAS u16* dst = (LAS u16*)(tr + seg * 528 + t * 32 + sch * 2);
      dst[0] = (u16)pk; dst[8] = (u16)(pk >> 16);
    }
    CBAR();
#pragma unroll
    for (int j = 0; j < 4; j++) {
      const int part = j & 1, token = (j >> 1) * 64 + lane;
      const v4u v = *(LAS v4u*)(tr + (token >> 4) * 528 + (token & 15) * 32 + part * 16);
      const int T16 = 8 * r + (token >> 4);
      __builtin_amdgcn_raw_buffer_store_b128(v, rsw, (int)WS_H + (((T16 >> 2) * KS2 + 56 + 2 * c + part) * 4 + (T16 & 3)) * 1024 + ((token & 15) + 16 * tg) * 16, 0, AUX_SC1);
    }
    waitv<0>();
    if (lds_arrive(CNT + 4) == 3u && lane == 0) __hip_atomic_store((GAS u64*)(ws + WS_FL + r * 256 + 128 + c * 8), tag, RLX_AGENT);
    __builtin_amdgcn_s_setprio(0);
    lds_wait(CNT + 5, 4u);
    issueB(3); issueB(4);
  }
  ST(8);
  v2u ex[4], ed; unsigned es[4][2];
  if (FFN) {
#pragma unroll
    for (int j = 0; j < 4; j++) ex[j] = *(const v2u*)(a.x + (size_t)(64 * rt + 16 * j + (tid >> 4)) * D_ + 64 * ct + 4 * (tid & 15));
    ed = *(const v2u*)(a.Dsk + 64 * ct + 4 * (tid & 15));
  }
  if (FFN) {
    if (tg == 0) { poll_flags(ws + WS_FL + (rt >> 1) * 256, 16, tag); lds_arrive(CNT + 6); }
    else lds_wait(CNT + 6, 1u);
  }
  else poll_flags(ws + WS_FL + (rt >> 1) * 256 + 128 + tg * 32, 4, tag);
  ST(9);
  const char* ap = ws + WS_H + ((size_t)rt * KS2 + s0) * 4096;
  if (FFN) {
#pragma unroll
    for (int j = 0; j < 4; j++) {
      const v2u t = __builtin_amdgcn_raw_buffer_load_b64(rsw, (int)WS_ST + (64 * rt + 16 * j + (tid >> 4)) * 8, 0, AUX_SC1);
      es[j][0] = t.x; es[j][1] = t.y;
    }
  }
  v4u ra[WA + 1][4];
#pragma unroll
  for (int i = 0; i < WA; i++) aload4(ra[i], lane * 16, ap + (size_t)i * 4096);
  if (FFN) lds_wait(CNT + 0, 4u);
  const int fw = (2 * g + (q >> 1)) & 3;
  unsigned toff[4];
#pragma unroll
  for (int j = 0; j < 4; j++) toff[j] = FFN ? (4 * g + q) * 128 + ((j ^ fw) << 5) + p4 * 8 : tok * 64 + ((g ^ ((-(tok >> 2)) & 3)) << 4) + j * 1024;
  f32x4 acc2[4][4];
#pragma unroll
  for (int i = 0; i < 4; i++)
#pragma unroll
    for (int j = 0; j < 4; j++) acc2[i][j] = f32x4{0, 0, 0, 0};
  W8 wf[2];
  auto rdw = [&](int i, int j) {
    const unsigned ad = (unsigned)(size_t)ringp(i) + toff[j];
    if (FFN) tr2k(wf[i & 1].h[2 * j], wf[i & 1].h[2 * j + 1], ad); else rd2h(wf[i & 1].h[2 * j], wf[i & 1].h[2 * j + 1], ad);
  };
#pragma unroll
  for (int j = 0; j < 4; j++) rdw(0, j);
  waitl_pin8(wf[0]);
  static_for<N2>([&](auto ic) {
    constexpr int i = decltype(ic)::value;
    waitv<p2_wait(i, N2, WA, RD)>();
    pin4(ra[i % (WA + 1)]);
    if (i == 0) ST(10);
    if (i == 4) ST(11);
    SB0();
    static_for<16>([&](auto mc) {
      constexpr int m = decltype(mc)::value, j = m >> 2, t = m & 3;
      const v4u wv = {wf[i & 1].h[2 * j].x, wf[i & 1].h[2 * j].y, wf[i & 1].h[2 * j + 1].x, wf[i & 1].h[2 * j + 1].y};
      acc2[j][t] = mfma(wv, ra[i % (WA + 1)][t], acc2[j][t]);
      SB0();
      if constexpr (m >= RDM && m < RDM + 4 && i + 1 < N2) rdw(i + 1, m - RDM);
      if constexpr (m >= GLM && m < GLM + 4 && i + RD < N2) glds16(FFN ? srcF(tg, i + RD, m - GLM) : srcS(tg, i + RD, m - GLM), ringp(i + RD) + (m - GLM) * 1024);
      if constexpr (m >= 8 && m < 12 && i + WA < N2) aload1<(m - 8) * 1024>(ra[(i + WA) % (WA + 1)][m - 8], lane * 16, ap + (size_t)(i + WA) * 4096);
      SB0();
    });
    keep8(wf[i & 1]);
    if constexpr (i + 1 < N2) waitl_pin8(wf[(i + 1) & 1]);
  });
  ST(12);
  auto pieceF = [&](int f, int t) { return L + (t < 3 ? 65536 + f * 12288 + t * 4096 : M_TR + f * 4096); };
  auto pieceS = [&](int f, int t) { return L + (t == 0 ? 114688 + f * 4096 : t < 3 ? 32768 + f * 8192 + (t - 1) * 4096 : f * 8192); };
  if (FFN) lds_wait(CNT + 4, 4u);
#pragma unroll
  for (int t = 0; t < 4; t++)
#pragma unroll
    for (int j = 0; j < 4; j++)
      *(LAS f32x4*)((FFN ? pieceF(tg, t) : pieceS(tg, t)) + (j * 4 + g) * 256 + ((tok + 4 * j + g) & 15) * 16) = acc2[j][t];
  lds_arrive(CNT + (FFN ? 8 : 9));
  if (!FFN) {
    if (b == 255 && w == 7) {
      poll_flags(ws + WS_FL + 15 * 256 + 128, 16, tag);
      if (lane == 0) __hip_atomic_store((GAS u64*)(ws + WS_EP), tag, RLX_AGENT);
    }
  } else {
    const __amdgpu_buffer_rsrc_t rso = __builtin_amdgcn_make_buffer_rsrc((void*)a.out, 0, 2048 * D_ * 4, 0x00020000);
    const int po = (tid & 15) * 256 + (((tid >> 4) + (tid & 15)) & 15) * 16;
    f32x4 sk[4], gg[4];
#pragma unroll
    for (int j = 0; j < 4; j++)
#pragma unroll
      for (int e = 0; e < 4; e++) {
        const unsigned xw = e < 2 ? ex[j].x : ex[j].y, dw = e < 2 ? ed.x : ed.y;
        const float xv = (e & 1) ? bfhi(xw) : bf2f(xw), dv = (e & 1) ? bfhi(dw) : bf2f(dw);
        sk[j][e] = rbf(mul_(rbf(fdiv(rbf(add_(xv, -__builtin_bit_cast(float, es[j][0]))), __builtin_bit_cast(float, es[j][1]))), dv));
      }
    lds_wait(CNT + 8, 4u);
#pragma unroll
    for (int j = 0; j < 4; j++) {
      const f32x4 p0 = *(LAS f32x4*)(pieceF(0, j) + po), p1 = *(LAS f32x4*)(pieceF(1, j) + po), p2 = *(LAS f32x4*)(pieceF(2, j) + po), p3 = *(LAS f32x4*)(pieceF(3, j) + po);
#pragma unroll
      for (int e = 0; e < 4; e++) gg[j][e] = rbf(silu_f(rbf(add_(add_(p0[e], p1[e]), add_(p2[e], p3[e])))));
    }
    lds_wait(CNT + 9, 4u);
    ST(13);
#pragma unroll
    for (int j = 0; j < 4; j++) {
      const f32x4 sy = (*(LAS f32x4*)(pieceS(0, j) + po) + *(LAS f32x4*)(pieceS(1, j) + po)) + (*(LAS f32x4*)(pieceS(2, j) + po) + *(LAS f32x4*)(pieceS(3, j) + po));
      const f32x4 xv = {bf2f(ex[j].x), bfhi(ex[j].x), bf2f(ex[j].y), bfhi(ex[j].y)};
      __builtin_amdgcn_raw_buffer_store_b128(__builtin_bit_cast(v4u, xv + (2.f * sy + sk[j]) * gg[j]), rso, ((64 * rt + 16 * j + (tid >> 4)) * D_ + 64 * ct + 4 * (tid & 15)) * 4, 0, 16);
    }
  }
  ST(14);
  ST_FLUSH(ws);
}

__global__ __launch_bounds__(512, 2) void k_fused(Args a) {
  extern __shared__ __attribute__((aligned(16))) char smem[];
  LAS char* L = (LAS char*)smem;
  const int w = __builtin_amdgcn_readfirstlane(threadIdx.x >> 6);
  u64 pc; asm volatile("s_getpc_b64 %0" : "=s"(pc));
  if (w >= 1 && w < 4) {
    const char* pb = (const char*)(pc & ~127ull);
    const unsigned o0 = (threadIdx.x - 64u) * 128u;
    touch(pb + o0, L + M_TR + 16896 + w * 256);
    touch(pb + (o0 + 24576u < CODE_BYTES ? o0 + 24576u : 0u), L + M_TR + 16896 + w * 256);
  }
  if (w < 4) body<true>(a, L, w);
  else body<false>(a, L, w);
}
}

extern "C" void kernel_launch(void* const* d_in, const int* in_sizes, int n_in, void* d_out, int out_size, void* d_ws, size_t ws_size,
                              hipStream_t stream) {
  Args a;
  a.x = (const u16*)d_in[0]; a.Lre = (const u16*)d_in[1]; a.Lim = (const u16*)d_in[2];
  a.Bre = (const u16*)d_in[3]; a.Bim = (const u16*)d_in[4]; a.Cre = (const u16*)d_in[5]; a.Cim = (const u16*)d_in[6];
  a.logdt = (const u16*)d_in[7]; a.Dsk = (const u16*)d_in[8]; a.w1 = (const u16*)d_in[10]; a.w2 = (const u16*)d_in[11];
  a.ws = (char*)d_ws; a.out = (float*)d_out;
  (void)hipFuncSetAttribute((const void*)k_fused, hipFuncAttributeMaxDynamicSharedMemorySize, LDS_BYTES);
  k_fused<<<256, 512, LDS_BYTES, stream>>>(a);
}
```

```cpp
#include <hip/hip_runtime.h>
#include <hip/hip_bf16.h>
#include <stdint.h>
#include <utility>

typedef unsigned short u16;
typedef unsigned long long u64;
typedef unsigned v4u __attribute__((ext_vector_type(4)));
typedef unsigned v2u __attribute__((ext_vector_type(2)));
typedef float f32x4 __attribute__((ext_vector_type(4)));
typedef float f32x2 __attribute__((ext_vector_type(2)));
typedef short bf16x8 __attribute__((ext_vector_type(8)));
typedef short v4s __attribute__((ext_vector_type(4)));
typedef __bf16 v2bf __attribute__((ext_vector_type(2)));
#define LAS __attribute__((address_space(3)))
#define GAS __attribute__((address_space(1)))
#define RLX_AGENT __ATOMIC_RELAXED, __HIP_MEMORY_SCOPE_AGENT
#define RLX_WG __ATOMIC_RELAXED, __HIP_MEMORY_SCOPE_WORKGROUP

namespace {
constexpr int D_ = 512, F_ = 1792;
constexpr int KS2 = 88;
constexpr size_t WS_H = 0;
constexpr size_t WS_H_BYTES = (size_t)32 * KS2 * 4096;
constexpr size_t WS_ST = WS_H + WS_H_BYTES;
constexpr size_t WS_E = WS_ST + 2048 * 8;
constexpr size_t WS_FL = WS_E + 16 * 512 * 16;
constexpr size_t WS_EP = WS_FL + 16 * 256;
constexpr size_t WS_END = WS_EP + 128;
constexpr int SLOT = 32768, OFF_W = 16384;
constexpr int M_STT = 131072, M_CS = M_STT + 1024, M_PRM = M_CS + 512, M_ST2 = M_PRM + 512, M_CNT = M_ST2 + 512, M_TR = M_CNT + 256;
constexpr int LDS_BYTES = 163840;
constexpr unsigned SPIN_CAP = 1u << 17;
#define FAUX 16
constexpr int AUX_SC1 = 16;
#define RDM 0
#define GLM 4
#define WSLEEP 4
#define W2AT 1
#define PSLEEP 6

__device__ __forceinline__ float bf2f(unsigned b) { return __builtin_bit_cast(float, b << 16); }
__device__ __forceinline__ float bfhi(unsigned b) { return __builtin_bit_cast(float, b & 0xffff0000u); }
__device__ __forceinline__ unsigned pkbf(float lo, float hi) {
  v2bf v; v.x = (__bf16)lo; v.y = (__bf16)hi; return __builtin_bit_cast(unsigned, v);
}
__device__ __forceinline__ float rbf(float f) { return bf2f(pkbf(f, 0.f)); }
__device__ __forceinline__ f32x4 mfma(v4u a, v4u b, f32x4 c) {
  return __builtin_amdgcn_mfma_f32_16x16x32_bf16(__builtin_bit_cast(bf16x8, a), __builtin_bit_cast(bf16x8, b), c, 0, 0, 0);
}
__device__ __forceinline__ float dot2(unsigned a, unsigned b, float c) {
  return __builtin_amdgcn_fdot2_f32_bf16(__builtin_bit_cast(v2bf, a), __builtin_bit_cast(v2bf, b), c, false);
}
__device__ __forceinline__ void glds16(const void* g, LAS char* l) {
  __builtin_amdgcn_global_load_lds((const GAS void*)g, (LAS void*)l, 16, 0, 0);
}
__device__ __forceinline__ void touch(const void* g, LAS char* scratch) { __builtin_amdgcn_global_load_lds((const GAS void*)g, (LAS void*)scratch, 4, 0, 0); }
__device__ __forceinline__ v2u trread(LAS char* p) {
  return __builtin_bit_cast(v2u, __builtin_amdgcn_ds_read_tr16_b64_v4i16((LAS v4s*)p));
}
__device__ __forceinline__ unsigned silu2(unsigned hb) {
  const f32x2 h = {bf2f(hb), bfhi(hb)};
  const f32x2 t = h * -1.44269504088896f;
  const f32x2 d = f32x2{__builtin_amdgcn_exp2f(t.x), __builtin_amdgcn_exp2f(t.y)} + 1.f;
  const f32x2 y = h * f32x2{__builtin_amdgcn_rcpf(d.x), __builtin_amdgcn_rcpf(d.y)};
  return pkbf(y.x, y.y);
}
struct cplx { float r, i; };
__device__ __forceinline__ float fma_(float a, float b, float c) { float d; asm("v_fma_f32 %0, %1, %2, %3" : "=v"(d) : "v"(a), "v"(b), "v"(c)); return d; }
__device__ __forceinline__ float fnma_(float a, float b, float c) { float d; asm("v_fma_f32 %0, -%1, %2, %3" : "=v"(d) : "v"(a), "v"(b), "v"(c)); return d; }
__device__ __forceinline__ float mul_(float a, float b) { float d; asm("v_mul_f32 %0, %1, %2" : "=v"(d) : "v"(a), "v"(b)); return d; }
__device__ __forceinline__ float add_(float a, float b) { float d; asm("v_add_f32 %0, %1, %2" : "=v"(d) : "v"(a), "v"(b)); return d; }
__device__ __forceinline__ float mul_t(float a, float b) { float d; asm("s_nop 0\n\tv_mul_f32 %0, %1, %2" : "=v"(d) : "v"(a), "v"(b)); return d; }
__device__ __forceinline__ float add_t(float a, float b) { float d; asm("s_nop 0\n\tv_add_f32 %0, %1, %2" : "=v"(d) : "v"(a), "v"(b)); return d; }
__device__ __forceinline__ float silu_f(float h) { return mul_t(h, __builtin_amdgcn_rcpf(add_t(1.f, __builtin_amdgcn_exp2f(mul_(h, -1.44269504088896f))))); }
__device__ __forceinline__ cplx cmul(cplx a, cplx b) { return {fnma_(a.i, b.i, mul_(a.r, b.r)), fma_(a.r, b.i, mul_(a.i, b.r))}; }
__device__ __forceinline__ cplx cfma(cplx a, cplx b, cplx c) {
  return {fma_(a.r, b.r, fnma_(a.i, b.i, c.r)), fma_(a.r, b.i, fma_(a.i, b.r, c.i))};
}
__device__ __forceinline__ unsigned hashA(unsigned a, unsigned b) { unsigned h = a * 0x9E3779B1u + (b ^ (b >> 15)) * 0x85EBCA77u; return h ^ (h >> 13); }
__device__ __forceinline__ unsigned hashB(unsigned a, unsigned b) { unsigned h = b * 0xC2B2AE3Du + (a ^ (a >> 16)) * 0x27D4EB2Fu; return h ^ (h >> 15); }
template <int N> __device__ __forceinline__ void waitv() { asm volatile("s_waitcnt vmcnt(%0)" ::"n"(N) : "memory"); }
#define WAITL0() __builtin_amdgcn_s_waitcnt(0xC07F)
#define BAR() __builtin_amdgcn_s_barrier()
#define SB0() __builtin_amdgcn_sched_barrier(0)
#define CBAR() asm volatile("" ::: "memory")
#define ST_DECL
#define ST(i)
#define SF(i)
#define ST_FLUSH(wsp)

__device__ __forceinline__ unsigned lds_arrive(LAS unsigned* c) {
  unsigned o = 0;
  if ((threadIdx.x & 63) == 0)
    asm volatile("s_waitcnt lgkmcnt(0)\n\tds_add_rtn_u32 %0, %1, %2\n\ts_waitcnt lgkmcnt(0)" : "=&v"(o) : "v"((unsigned)(size_t)c), "v"(1u) : "memory");
  return __builtin_amdgcn_readfirstlane(o);
}
__device__ __forceinline__ void lds_wait(LAS unsigned* c, unsigned want) {
#pragma nounroll
  for (unsigned spins = 0; spins < (1u << 22); ++spins) {
    unsigned v;
    asm volatile("ds_read_b32 %0, %1\n\ts_waitcnt lgkmcnt(0)" : "=&v"(v) : "v"((unsigned)(size_t)c) : "memory");
    if (__builtin_amdgcn_readfirstlane(v) >= want) break;
    __builtin_amdgcn_s_sleep(1);
  }
}
struct Tr14 { v2u h[14]; };
template <int OFF> __device__ __forceinline__ void tr14(Tr14& t, const unsigned (&ad)[7]) {
  asm volatile("ds_read_b64_tr_b16 %0, %14 offset:%21\n\tds_read_b64_tr_b16 %1, %14 offset:%22\n\t"
               "ds_read_b64_tr_b16 %2, %15 offset:%21\n\tds_read_b64_tr_b16 %3, %15 offset:%22\n\t"
               "ds_read_b64_tr_b16 %4, %16 offset:%21\n\tds_read_b64_tr_b16 %5, %16 offset:%22\n\t"
               "ds_read_b64_tr_b16 %6, %17 offset:%21\n\tds_read_b64_tr_b16 %7, %17 offset:%22\n\t"
               "ds_read_b64_tr_b16 %8, %18 offset:%21\n\tds_read_b64_tr_b16 %9, %18 offset:%22\n\t"
               "ds_read_b64_tr_b16 %10, %19 offset:%21\n\tds_read_b64_tr_b16 %11, %19 offset:%22\n\t"
               "ds_read_b64_tr_b16 %12, %20 offset:%21\n\tds_read_b64_tr_b16 %13, %20 offset:%22"
               : "=&v"(t.h[0]), "=&v"(t.h[1]), "=&v"(t.h[2]), "=&v"(t.h[3]), "=&v"(t.h[4]), "=&v"(t.h[5]), "=&v"(t.h[6]),
                 "=&v"(t.h[7]), "=&v"(t.h[8]), "=&v"(t.h[9]), "=&v"(t.h[10]), "=&v"(t.h[11]), "=&v"(t.h[12]), "=&v"(t.h[13])
               : "v"(ad[0]), "v"(ad[1]), "v"(ad[2]), "v"(ad[3]), "v"(ad[4]), "v"(ad[5]), "v"(ad[6]), "n"(OFF), "n"(OFF + 1024)
               : "memory");
}
template <int OFF> __device__ __forceinline__ void rd2x(v4u (&x)[2], unsigned ad) {
  asm volatile("ds_read_b128 %0, %2 offset:%3\n\tds_read_b128 %1, %2 offset:%4" : "=&v"(x[0]), "=&v"(x[1]) : "v"(ad), "n"(OFF), "n"(OFF + 2048) : "memory");
}
template <int N> __device__ __forceinline__ void waitl_pin(Tr14& t, v4u (&x)[2]) {
  asm volatile("s_waitcnt lgkmcnt(%16)"
               : "+v"(t.h[0]), "+v"(t.h[1]), "+v"(t.h[2]), "+v"(t.h[3]), "+v"(t.h[4]), "+v"(t.h[5]), "+v"(t.h[6]), "+v"(t.h[7]), "+v"(t.h[8]),
                 "+v"(t.h[9]), "+v"(t.h[10]), "+v"(t.h[11]), "+v"(t.h[12]), "+v"(t.h[13]), "+v"(x[0]), "+v"(x[1])
               : "n"(N) : "memory");
}
template <int OFF> __device__ __forceinline__ void tr2(v2u& h0, v2u& h1, unsigned ad) {
  asm volatile("ds_read_b64_tr_b16 %0, %2 offset:%3\n\tds_read_b64_tr_b16 %1, %2 offset:%4" : "=&v"(h0), "=&v"(h1) : "v"(ad), "n"(OFF), "n"(OFF + 1024) : "memory");
}
__device__ __forceinline__ void keep(const Tr14& t, const v4u (&x)[2]) {
  asm volatile("" :: "v"(t.h[0]), "v"(t.h[1]), "v"(t.h[2]), "v"(t.h[3]), "v"(t.h[4]), "v"(t.h[5]), "v"(t.h[6]), "v"(t.h[7]), "v"(t.h[8]),
               "v"(t.h[9]), "v"(t.h[10]), "v"(t.h[11]), "v"(t.h[12]), "v"(t.h[13]), "v"(x[0]), "v"(x[1]));
}
__device__ __forceinline__ void waitl_pinS(v4u (&x)[2], v2u (&t)[4]) {
  asm volatile("s_waitcnt lgkmcnt(0)" : "+v"(x[0]), "+v"(x[1]), "+v"(t[0]), "+v"(t[1]), "+v"(t[2]), "+v"(t[3]) :: "memory");
}
template <int OFF> __device__ __forceinline__ void tr4(v2u (&h)[4], unsigned a0, unsigned a1) {
  asm volatile("ds_read_b64_tr_b16 %0, %4 offset:%6\n\tds_read_b64_tr_b16 %1, %4 offset:%7\n\t"
               "ds_read_b64_tr_b16 %2, %5 offset:%6\n\tds_read_b64_tr_b16 %3, %5 offset:%7"
               : "=&v"(h[0]), "=&v"(h[1]), "=&v"(h[2]), "=&v"(h[3]) : "v"(a0), "v"(a1), "n"(OFF), "n"(OFF + 1024) : "memory");
}
template <int OFF> __device__ __forceinline__ void rd4w(v4u (&x)[4], unsigned ad) {
  asm volatile("ds_read_b128 %0, %4 offset:%5\n\tds_read_b128 %1, %4 offset:%6\n\tds_read_b128 %2, %4 offset:%7\n\tds_read_b128 %3, %4 offset:%8"
               : "=&v"(x[0]), "=&v"(x[1]), "=&v"(x[2]), "=&v"(x[3]) : "v"(ad), "n"(OFF), "n"(OFF + 2048), "n"(OFF + 4096), "n"(OFF + 6144) : "memory");
}
template <int OFF> __device__ __forceinline__ void rd1(v4u& x, unsigned ad) {
  asm volatile("ds_read_b128 %0, %1 offset:%2" : "=&v"(x) : "v"(ad), "n"(OFF) : "memory");
}
template <int N> __device__ __forceinline__ void waitl_pin5(v4u (&x)[4], v4u& y) {
  asm volatile("s_waitcnt lgkmcnt(%5)" : "+v"(x[0]), "+v"(x[1]), "+v"(x[2]), "+v"(x[3]), "+v"(y) : "n"(N) : "memory");
}
template <int N> __device__ __forceinline__ void waitl_pin4(v4u (&x)[4]) {
  asm volatile("s_waitcnt lgkmcnt(%4)" : "+v"(x[0]), "+v"(x[1]), "+v"(x[2]), "+v"(x[3]) : "n"(N) : "memory");
}
__device__ __forceinline__ float fdiv(float a, float b) {
  const float rc = __builtin_amdgcn_rcpf(b), q0 = mul_t(a, rc);
  return fma_(fnma_(q0, b, a), rc, q0);
}
typedef std::integral_constant<int, 0> I0;
#define IC(n) std::integral_constant<int, (n)>{}
#define CAS __attribute__((address_space(4)))
__device__ __forceinline__ void wload(unsigned& d, const char* p) { asm volatile("global_load_dword %0, %1, off" : "=&v"(d) : "v"(p) : "memory"); }
struct W8 { v2u h[8]; };
__device__ __forceinline__ void tr2k(v2u& h0, v2u& h1, unsigned ad) {
  asm volatile("ds_read_b64_tr_b16 %0, %2\n\tds_read_b64_tr_b16 %1, %2 offset:2048" : "=&v"(h0), "=&v"(h1) : "v"(ad) : "memory");
}
__device__ __forceinline__ void rd2h(v2u& h0, v2u& h1, unsigned ad) {
  asm volatile("ds_read_b64 %0, %2\n\tds_read_b64 %1, %2 offset:8" : "=&v"(h0), "=&v"(h1) : "v"(ad) : "memory");
}
__device__ __forceinline__ void waitl_pin8(W8& t) {
  asm volatile("s_waitcnt lgkmcnt(0)" : "+v"(t.h[0]), "+v"(t.h[1]), "+v"(t.h[2]), "+v"(t.h[3]), "+v"(t.h[4]), "+v"(t.h[5]), "+v"(t.h[6]), "+v"(t.h[7]) :: "memory");
}
__device__ __forceinline__ void keep8(const W8& t) {
  asm volatile("" :: "v"(t.h[0]), "v"(t.h[1]), "v"(t.h[2]), "v"(t.h[3]), "v"(t.h[4]), "v"(t.h[5]), "v"(t.h[6]), "v"(t.h[7]));
}
__device__ __forceinline__ void pin4(v4u (&x)[4]) { asm volatile("" : "+v"(x[0]), "+v"(x[1]), "+v"(x[2]), "+v"(x[3])); }
template <int OFF> __device__ __forceinline__ void aload1(v4u& r, unsigned voff, const char* sbase) {
  asm volatile("global_load_dwordx4 %0, %1, %2 offset:%3 sc1" : "=&v"(r) : "v"(voff), "s"(sbase), "n"(OFF) : "memory");
}
__device__ __forceinline__ void poll_flags(const char* p, int n, u64 tag) {
  const int lane = threadIdx.x & 63;
  GAS u64* f = (GAS u64*)p + (lane < n ? lane : 0);
#pragma nounroll
  for (unsigned spins = 0; spins < SPIN_CAP; ++spins) {
    if (__all(__hip_atomic_load(f, RLX_AGENT) == tag)) break;
    __builtin_amdgcn_s_sleep(PSLEEP);
  }
  __builtin_amdgcn_fence(__ATOMIC_ACQUIRE, "wavefront");
}
__device__ __forceinline__ void aload4(v4u (&r)[4], unsigned voff, const char* sbase) {
  asm volatile("global_load_dwordx4 %0, %4, %5 sc1\n\tglobal_load_dwordx4 %1, %4, %5 offset:1024 sc1\n\t"
               "global_load_dwordx4 %2, %4, %5 offset:2048 sc1\n\tglobal_load_dwordx4 %3, %4, %5 offset:3072 sc1"
               : "=&v"(r[0]), "=&v"(r[1]), "=&v"(r[2]), "=&v"(r[3]) : "v"(voff), "s"(sbase) : "memory");
}
__device__ __forceinline__ void trread8(v4u (&wf)[4], unsigned a0, unsigned a1, unsigned a2, unsigned a3) {
  v2u x0, x1, x2, x3, x4, x5, x6, x7;
  asm volatile("ds_read_b64_tr_b16 %0, %8\n\tds_read_b64_tr_b16 %1, %8 offset:2048\n\t"
               "ds_read_b64_tr_b16 %2, %9\n\tds_read_b64_tr_b16 %3, %9 offset:2048\n\t"
               "ds_read_b64_tr_b16 %4, %10\n\tds_read_b64_tr_b16 %5, %10 offset:2048\n\t"
               "ds_read_b64_tr_b16 %6, %11\n\tds_read_b64_tr_b16 %7, %11 offset:2048\n\ts_waitcnt lgkmcnt(0)"
               : "=&v"(x0), "=&v"(x1), "=&v"(x2), "=&v"(x3), "=&v"(x4), "=&v"(x5), "=&v"(x6), "=&v"(x7)
               : "v"(a0), "v"(a1), "v"(a2), "v"(a3) : "memory");
  wf[0] = v4u{x0.x, x0.y, x1.x, x1.y}; wf[1] = v4u{x2.x, x2.y, x3.x, x3.y};
  wf[2] = v4u{x4.x, x4.y, x5.x, x5.y}; wf[3] = v4u{x6.x, x6.y, x7.x, x7.y};
}
constexpr int p2_wait(int i, int N, int W, int RD) {
  int total = 0, need = 0;
  for (int j = 0; j < W && j < N; j++) { total += 4; if (j == i) need = total; }
  for (int s = 0; s < i; s++) {
    if (s + RD < N) { total += 4; if (s + RD == i + 1) need = total; }
    if (s + W < N) { total += 4; if (s + W == i) need = total > need ? total : need; }
  }
  return total - need;
}

template <int... I, class Fn> __device__ __forceinline__ void static_for_(std::integer_sequence<int, I...>, Fn&& f) { (f(std::integral_constant<int, I>{}), ...); }
template <int N, class Fn> __device__ __forceinline__ void static_for(Fn&& f) { static_for_(std::make_integer_sequence<int, N>{}, f); }

struct Args {
  const u16 *x, *Lre, *Lim, *Bre, *Bim, *Cre, *Cim, *logdt, *Dsk, *w1, *w2;
  char* ws; float* out;
};

template <bool FFN>
__device__ __forceinline__ void body(const Args& a, LAS char* L, const int w) {
  const int b = blockIdx.x, xcd = b & 7, idx = b >> 3;
  const int r = (xcd & 1) * 8 + (idx & 7), c = (xcd >> 1) * 4 + (idx >> 3);
  const int rt = 4 * xcd + (idx & 3), ct = idx >> 2;
  const int tid = threadIdx.x, lane = tid & 63, tok = lane & 15, g = lane >> 4, tg = w & 3;
  const int codd = c & 1;
  char* const ws = a.ws;
  LAS unsigned* CNT = (LAS unsigned*)(L + M_CNT);
  ST_DECL ST(0);
  const __amdgpu_buffer_rsrc_t rsw = __builtin_amdgcn_make_buffer_rsrc((void*)ws, 0, (int)WS_END, 0x00020000);
  const int r8 = lane >> 3, p8 = lane & 7;
  constexpr int N2 = FFN ? 14 : 8;
  const int s0 = FFN ? 14 * tg : 56 + 8 * tg;
  const char* bw0 = (const char*)(a.w2 + 64 * ct);
  const unsigned bwl = (r8 * D_ + (((((p8 >> 1) ^ ((r8 >> 1) & 3)) << 1) | (p8 & 1)) << 3)) * 2;
  const int r4 = lane >> 2, c4 = lane & 3;
  const size_t bc0 = (size_t)(64 * ct) * 1024;
  const unsigned bcl = (r4 * 512 + ((c4 ^ ((-(r4 >> 2)) & 3)) << 3)) * 2;
  constexpr int RD = FFN ? 3 : 5, WA = FFN ? 4 : 7;
  auto ringp = [&](int i) {
    const int e = i % RD;
    return L + (FFN ? 65536 + tg * 12288 + e * 4096 : e == 0 ? 114688 + tg * 4096 : e < 3 ? 32768 + tg * 8192 + (e - 1) * 4096 : tg * 8192 + (e - 3) * 4096);
  };
  auto srcF = [&](int f, int i, int j) { return bw0 + (size_t)(32 * (14 * f + i) + 8 * j) * (D_ * 2) + bwl; };
  auto srcS = [&](int f, int i, int j) { return (const char*)((i & 1) ? a.Cim : a.Cre) + bc0 + 256 * f + 64 * (i >> 1) + (size_t)j * 16 * 1024 + bcl; };
  auto issueB = [&](int i) {
    LAS char* sl = ringp(i);
#pragma unroll
    for (int j = 0; j < 4; j++) glds16(FFN ? srcF(tg, i, j) : srcS(tg, i, j), sl + j * 1024);
  };
  const int swz = (tok >> 1) & 7;
  const int xoff = (32 * tg + tok) * 128 + ((g ^ swz) << 4);
  const int q = tok >> 2, p4 = lane & 3, fl = q | ((g & 1) << 2);
  const unsigned lb = (unsigned)(size_t)L;
  const unsigned xad[4] = {lb + xoff, lb + (xoff ^ 64), lb + xoff + 2 * SLOT, lb + (xoff ^ 64) + 2 * SLOT};
  auto waddr = [&](int v) { const int pi = codd ? (v == 6 ? 1 : v + 2) : v; return lb + OFF_W + (8 * g + q) * 256 + ((pi ^ fl) << 5) + p4 * 8; };
  constexpr unsigned ONES = 0x3f803f80u;
  LAS f32x2* STT = (LAS f32x2*)(L + M_STT);
  LAS float* CS = (LAS float*)(L + M_CS);
  u64 tag;

  if (FFN) {
    tag = *(const CAS u64*)(ws + WS_EP) + 1ull;
    if (tid < 16) CNT[tid] = 0u;
    LAS char* junk = L + M_TR + 16896 + tg * 256;
    if (w == 0 && lane < 32) {
      const float lr = bf2f(a.Lre[32 * c + lane]), li = bf2f(a.Lim[32 * c + lane]), dt = rbf(expf(bf2f(a.logdt[32 * c + lane])));
      const float er = expf(dt * lr); float sn, cn; sincosf(dt * li, &sn, &cn);
      const float br = er * cn, bi = er * sn, den = 1.f / (lr * lr + li * li), nr = br - 1.f;
      *(LAS f32x4*)(L + M_PRM + lane * 16) = f32x4{br, bi, (nr * lr + bi * li) * den, (bi * lr - nr * li) * den};
    }
    if (tid < 152) {
      const int t = b * 152 + tid;
      const char* p = t < 14336 ? (const char*)a.w1 + (size_t)t * 128
                    : t < 30720 ? (const char*)a.x + (size_t)(t - 14336) * 128
                    : t < 34816 ? (const char*)a.Bre + (size_t)(t - 30720) * 128 : (const char*)a.Bim + (size_t)(t - 34816) * 128;
      __builtin_amdgcn_s_sleep(WSLEEP);
      touch(p, junk);
    }
    const int t2 = b * 88 + (tid < 88 ? tid : 0);
    const char* wp2 = t2 < 14336 ? (const char*)a.w2 + (size_t)t2 * 128
                    : t2 < 18432 ? (const char*)a.Cre + (size_t)(t2 - 14336) * 128 : (const char*)a.Cim + (size_t)(t2 - 18432) * 128;
    unsigned wlo[7], whi[7];
#pragma unroll
    for (int v = 0; v < 7; v++) { wlo[v] = waddr(v); whi[v] = wlo[v] + 2 * SLOT; }
    f32x4 acc[7][2];
#pragma unroll
    for (int u = 0; u < 7; u++) { acc[u][0] = f32x4{0, 0, 0, 0}; acc[u][1] = f32x4{0, 0, 0, 0}; }
    v4u xf[2][2]; Tr14 wt[2];
    auto rd = [&](auto kc) {
      constexpr int ks = decltype(kc)::value, kk = ks & 1, sl = (ks >> 1) & 3;
      rd2x<(sl & 1) * SLOT>(xf[kk], xad[(sl >> 1) * 2 + kk]);
      tr14<(sl & 1) * SLOT + kk * 8192>(wt[kk], (sl >> 1) ? whi : wlo);
    };
    auto step = [&](auto kc) {
      constexpr int ks = decltype(kc)::value, kk = ks & 1, kn = ks + 1, nk = kn & 1, sl = (kn >> 1) & 3;
      static_for<14>([&](auto mc) {
        constexpr int m = decltype(mc)::value, v = m >> 1, tt = m & 1;
        const v4u wv = {wt[kk].h[2 * v].x, wt[kk].h[2 * v].y, wt[kk].h[2 * v + 1].x, wt[kk].h[2 * v + 1].y};
        acc[v][tt] = mfma(wv, xf[kk][tt], acc[v][tt]);
        SB0();
        if constexpr (kn < 16) {
          if (m == 0) rd2x<(sl & 1) * SLOT>(xf[nk], xad[(sl >> 1) * 2 + nk]);
          if constexpr (m >= 1 && m <= 7) tr2<(sl & 1) * SLOT + nk * 8192>(wt[nk].h[2 * (m - 1)], wt[nk].h[2 * (m - 1) + 1], (sl >> 1) ? whi[m - 1] : wlo[m - 1]);
          SB0();
        }
      });
      keep(wt[kk], xf[kk]);
      if constexpr (kn < 16) waitl_pin<0>(wt[nk], xf[nk]);
    };
    WAITL0(); BAR();
    ST(1);
    rd(IC(0));
    waitl_pin<0>(wt[0], xf[0]);
    static_for<8>([&](auto sc) {
      constexpr int s = decltype(sc)::value;
      SF(1);
      step(IC(2 * s));
      SF(5);
      BAR();
      SF(6);
      if (s == 3) ST(2);
      if (s == W2AT && tg < 2) touch(wp2, junk);
      step(IC(2 * s + 1));
      SF(8);
    });
    ST(3);
    BAR();
    ST(4);
    {
      __builtin_amdgcn_s_setprio(3);
      const f32x2 st0 = STT[32 * tg + tok], st1 = STT[32 * tg + 16 + tok];
      f32x4 cs[7];
#pragma unroll
      for (int v = 0; v < 7; v++) cs[v] = *(LAS f32x4*)(CS + v * 16 + 4 * g);
      const int T16 = 8 * r + 2 * tg;
      const int fb = (int)WS_H + ((T16 >> 2) * KS2 * 4 + (T16 & 3)) * 1024 + lane * 16;
      const float km[2] = {-st0.x * st0.y, -st1.x * st1.y}, rs[2] = {st0.y, st1.y};
      v2u o[2][2];
#pragma unroll
      for (int v = 0; v < 7; v++) {
#pragma unroll
        for (int tt = 0; tt < 2; tt++) {
          unsigned pk[2];
#pragma unroll
          for (int hh = 0; hh < 2; hh++) {
            const f32x2 ac = {acc[v][tt][2 * hh], acc[v][tt][2 * hh + 1]}, cc = {cs[v][2 * hh], cs[v][2 * hh + 1]};
            const f32x2 u = __builtin_elementwise_fma(ac, f32x2{rs[tt], rs[tt]}, cc * km[tt]);
            unsigned hb = pkbf(u.x, u.y);
            asm volatile("" : "+v"(hb));
            pk[hh] = silu2(hb);
          }
          o[v & 1][tt] = v2u{pk[0], pk[1]};
        }
        if (v & 1) {
          const int U = 7 * c + v - 1 + codd;
#pragma unroll
          for (int tt = 0; tt < 2; tt++)
            __builtin_amdgcn_raw_buffer_store_b128(v4u{o[0][tt].x, o[0][tt].y, o[1][tt].x, o[1][tt].y}, rsw, fb + tt * 1024 + (U >> 1) * 4096, 0, FAUX);
        } else if (v == 6) {
          const int U = codd ? 7 * c : 7 * c + 6;
#pragma unroll
          for (int tt = 0; tt < 2; tt++)
            __builtin_amdgcn_raw_buffer_store_b64(o[0][tt], rsw, fb + tt * 1024 + (U >> 1) * 4096 + (U & 1) * 8, 0, FAUX);
        }
      }
    }
    ST(5);
    lds_arrive(CNT + 7);
    waitv<0>();
    if (lds_arrive(CNT + 3) == 3u) {
      lds_wait(CNT + 0, 4u);
      if (lane == 0) __hip_atomic_store((GAS u64*)(ws + WS_FL + r * 256 + c * 8), tag, RLX_AGENT);
    }
    ST(6);
    __builtin_amdgcn_s_setprio(0);
  } else {
    unsigned sx[2], sw[2];
#pragma unroll
    for (int h = 0; h < 2; h++) {
      sx[h] = ((8 * h + r8) * D_ + ((p8 ^ ((4 * h + (r8 >> 1)) & 7)) << 3)) * 2;
      sw[h] = ((8 * h + g) * F_ + (((((tok >> 1) ^ (g | (h << 2))) << 1) | (tok & 1)) << 3)) * 2;
    }
    const char* xb = (const char*)(a.x + (size_t)(128 * r + 32 * tg) * D_);
    const char* wb1 = (const char*)(a.w1 + (size_t)(16 * tg) * F_ + 112 * c - 16 * codd);
    const int rot = idx & 7;
    auto issue1 = [&](int s, int j) {
      LAS char* sb = L + (s & 3) * SLOT;
      const int k0 = ((s + rot) & 7) * 64;
      if (j < 4) glds16(xb + ((j >> 1) * 16 * D_ + k0) * 2 + sx[j & 1], sb + (4 * tg + j) * 1024);
      else glds16(wb1 + (size_t)(k0 + (j & 1) * 4) * F_ * 2 + sw[(j - 4) >> 1], sb + OFF_W + (4 * tg + j - 4) * 1024);
    };
    auto issue = [&](int s) {
#pragma unroll
      for (int j = 0; j < 8; j++) issue1(s, j);
    };
    issue(0); issue(1);
    __builtin_amdgcn_s_setprio(3);
    tag = *(const CAS u64*)(ws + WS_EP) + 1ull;
    const u16* ssp[2];
#pragma unroll
    for (int h = 0; h < 2; h++) {
      const int n = 16 * tg + 8 * h + r8;
      ssp[h] = ((n & 1) ? a.Bim : a.Bre) + (size_t)(32 * c + (n >> 1)) * D_ + ((p8 ^ ((n >> 1) & 7)) << 3);
    }
    auto issueS1 = [&](int js, int j) { glds16(ssp[j & 1] + ((4 * js + (j >> 1) + rot) & 7) * 64, L + js * SLOT + (j >> 1) * 8192 + (2 * tg + (j & 1)) * 1024); };
    auto issueR1 = [&](int i, int j) {
      const int o = 32 * i + 8 * tg + j;
      if (o < 48) glds16(srcF(o / 12, (o % 12) >> 2, o & 3), L + 65536 + o * 1024);
      else glds16(srcS((o - 48) >> 2, 0, o & 3), L + 65536 + o * 1024);
    };
    auto piece = [&](int s, int j) {
      if (s <= 3) issue1(s + 4, j); else if (s <= 5) issueS1(s - 4, j); else issueR1(s - 6, j);
    };
    const unsigned wa0 = waddr(tg), wa1 = waddr(tg == 3 ? 3 : tg + 4);
    const v4u ones = {ONES, ONES, ONES, ONES};
    v4u xr[16][2];
    v2u wq[2][4];
    f32x4 sm[2] = {{0, 0, 0, 0}, {0, 0, 0, 0}}, sq[2] = {{0, 0, 0, 0}, {0, 0, 0, 0}}, cf[2] = {{0, 0, 0, 0}, {0, 0, 0, 0}};
    auto rdS = [&](auto kc) {
      constexpr int ks = decltype(kc)::value, kk = ks & 1, sl = (ks >> 1) & 3;
      rd2x<(sl & 1) * SLOT>(xr[ks], xad[(sl >> 1) * 2 + kk]);
      tr4<(sl & 1) * SLOT + kk * 8192>(wq[kk], wa0 + (sl >> 1) * 2 * SLOT, wa1 + (sl >> 1) * 2 * SLOT);
    };
    auto pinS = [&](auto kc) {
      constexpr int ks = decltype(kc)::value, kk = ks & 1;
      waitl_pinS(xr[ks], wq[kk]);
    };
    auto stat = [&](auto kc, int s, int j0) {
      constexpr int ks = decltype(kc)::value, kk = ks & 1;
      sm[0] = mfma(ones, xr[ks][0], sm[0]); sq[0] = mfma(xr[ks][0], xr[ks][0], sq[0]);
      SB0(); if (j0 >= 0) piece(s, j0); SB0();
      sm[1] = mfma(ones, xr[ks][1], sm[1]);
      SB0(); if (j0 >= 0) piece(s, j0 + 1); SB0();
      sq[1] = mfma(xr[ks][1], xr[ks][1], sq[1]);
      SB0(); if (j0 >= 0) piece(s, j0 + 2); SB0();
      cf[0] = mfma(v4u{wq[kk][0].x, wq[kk][0].y, wq[kk][1].x, wq[kk][1].y}, ones, cf[0]);
      SB0(); if (j0 >= 0) piece(s, j0 + 3); SB0();
      cf[1] = mfma(v4u{wq[kk][2].x, wq[kk][2].y, wq[kk][3].x, wq[kk][3].y}, ones, cf[1]);
      SB0();
    };
    const int sch = lane & 7, seg = lane >> 3, chg = 32 * c + 8 * tg + sch;
    cplx lam, coef, l16, l32, l64, lseg = {1.f, 0.f}, wa = {0.f, 0.f}, wb = {0.f, 0.f};
    waitv<8>();
    BAR();
    ST(1);
    rdS(IC(0));
    issue(2);
    pinS(IC(0));
    static_for<8>([&](auto sc) {
      constexpr int s = decltype(sc)::value;
      SF(1);
      rdS(IC(2 * s + 1));
      if (s == 0) { stat(IC(0), -1, 0); issue1(3, 4); issue1(3, 5); issue1(3, 6); issue1(3, 7); }
      else stat(IC(2 * s), s - 1, 4);
      pinS(IC(2 * s + 1));
      SF(2);
      if (s <= 6) waitv<16>();
      SF(3);
      BAR();
      SF(6);
      if (s == 3) ST(2);
      if constexpr (s < 7) rdS(IC(2 * s + 2));
      stat(IC(2 * s + 1), s, 0);
      if constexpr (s < 7) pinS(IC(2 * s + 2));
      SF(8);
      if (s == 1) {
        const f32x4 pr = *(LAS f32x4*)(L + M_PRM + (8 * tg + sch) * 16);
        lam = {pr[0], pr[1]}; coef = {pr[2], pr[3]};
        const cplx l2 = cmul(lam, lam), l4 = cmul(l2, l2), l8 = cmul(l4, l4);
        l16 = cmul(l8, l8); l32 = cmul(l16, l16); l64 = cmul(l32, l32);
        const cplx l128 = cmul(l64, l64), l256 = cmul(l128, l128), l512 = cmul(l256, l256), l1024 = cmul(l512, l512);
        if (seg & 1) lseg = l16;
        if (seg & 2) lseg = cmul(lseg, l32);
        if (seg & 4) lseg = cmul(lseg, l64);
        const int nb = r - 1 - (seg + 8);
        cplx t = {1.f, 0.f};
        const int ne = nb >= 0 ? nb : (nb + 8 >= 0 ? nb + 8 : 0);
        if (ne & 1) t = l128;
        if (ne & 2) t = cmul(t, l256);
        if (ne & 4) t = cmul(t, l512);
        if (nb >= 0) { wb = t; wa = cmul(t, l1024); } else if (nb + 8 >= 0) { wa = t; }
        asm volatile("" : "+v"(wa.r), "+v"(wa.i), "+v"(wb.r), "+v"(wb.i), "+v"(lseg.r), "+v"(lseg.i));
      }
    });
    {
      const int e = tok & 3;
      const float d0 = e == 0 ? sq[0][0] : e == 1 ? sq[0][1] : e == 2 ? sq[0][2] : sq[0][3];
      const float d1 = e == 0 ? sq[1][0] : e == 1 ? sq[1][1] : e == 2 ? sq[1][2] : sq[1][3];
      const bool own = g == q;
#pragma unroll
      for (int tt = 0; tt < 2; tt++) {
        float m = sm[tt][0] * (1.f / 512.f);
        asm volatile("" : "+v"(m));
        const float var = fmaxf(fnma_(m, m, mul_(tt ? d1 : d0, 1.f / 512.f)), 0.f);
        const float mb = rbf(m), sd = rbf(__builtin_amdgcn_sqrtf(rbf(rbf(var) + 1e-5f)));
        if (own) {
          STT[32 * tg + 16 * tt + tok] = f32x2{mb, fdiv(1.f, sd)};
          if (c == 0)
            __builtin_amdgcn_raw_buffer_store_b64(v2u{__builtin_bit_cast(unsigned, mb), __builtin_bit_cast(unsigned, sd)}, rsw,
                                                  (int)WS_ST + (128 * r + 32 * tg + 16 * tt + tok) * 8, 0, AUX_SC1);
        }
      }
      if (tok == 0) {
        *(LAS f32x4*)(CS + tg * 16 + 4 * g) = cf[0];
        if (tg < 3) *(LAS f32x4*)(CS + (tg + 4) * 16 + 4 * g) = cf[1];
      }
    }
    ST(3);
    WAITL0(); BAR();
    ST(4);
#pragma unroll
    for (int j = 4; j < 8; j++) piece(7, j);
    __builtin_amdgcn_s_setprio(0);
    f32x4 acc[4][2];
    f32x4 cs4 = {0, 0, 0, 0};
#pragma unroll
    for (int u = 0; u < 4; u++) { acc[u][0] = f32x4{0, 0, 0, 0}; acc[u][1] = f32x4{0, 0, 0, 0}; }
    const unsigned sad[2] = {lb + tok * 128 + ((g ^ swz) << 4), lb + ((tok * 128 + ((g ^ swz) << 4)) ^ 64)};
    v4u wf[3][4], wo[3];
    const unsigned oad[2] = {sad[0] + tg * 2048, sad[1] + tg * 2048};
    waitv<0>();
    lds_arrive(CNT + 0); lds_wait(CNT + 0, 4u);
    rd4w<0>(wf[0], sad[0]); rd1<0>(wo[0], oad[0]);
    rd4w<0>(wf[1], sad[1]); rd1<0>(wo[1], oad[1]);
    lds_wait(CNT + 7, 4u);
    static_for<16>([&](auto kc) {
      constexpr int ks = decltype(kc)::value, kn = ks + 2;
      if constexpr (kn < 16) {
        rd4w<(kn >> 3) * SLOT + ((kn >> 1) & 3) * 8192>(wf[kn % 3], sad[kn & 1]);
        rd1<(kn >> 3) * SLOT + ((kn >> 1) & 3) * 8192>(wo[kn % 3], oad[kn & 1]);
        waitl_pin5<10>(wf[ks % 3], wo[ks % 3]);
      } else if constexpr (kn == 16) waitl_pin5<5>(wf[ks % 3], wo[ks % 3]);
      else waitl_pin5<0>(wf[ks % 3], wo[ks % 3]);
      SB0();
#pragma unroll
      for (int u = 0; u < 4; u++) { acc[u][0] = mfma(wf[ks % 3][u], xr[ks][0], acc[u][0]); acc[u][1] = mfma(wf[ks % 3][u], xr[ks][1], acc[u][1]); }
      cs4 = mfma(wo[ks % 3], ones, cs4);
      if (ks == 8) lds_arrive(CNT + 1);
      SB0();
    });
    ST(5);
    __builtin_amdgcn_s_setprio(3);
    lds_wait(CNT + 1, 4u);
#pragma unroll
    for (int u = 0; u < 4; u++)
#pragma unroll
      for (int tt = 0; tt < 2; tt++) {
        const int token = 32 * tg + 16 * tt + tok;
        *(LAS f32x4*)(L + token * 256 + (((4 * u + g) ^ (token & 7) ^ (((token >> 4) & 3) << 2)) << 4)) = acc[u][tt];
      }
    const float ca = lane & 1 ? cs4[2] : cs4[0], cb = lane & 1 ? cs4[3] : cs4[1];
    const f32x2 c2 = {__shfl(ca, (sch >> 1) * 16 + (sch & 1)), __shfl(cb, (sch >> 1) * 16 + (sch & 1))};
    lds_arrive(CNT + 2);
    lds_wait(CNT + 2, 4u);
    cplx bu[16], h = {0.f, 0.f}, init0 = {0.f, 0.f};
    asm volatile("" : "+v"(l16.r), "+v"(l16.i));
    l32 = cmul(l16, l16); l64 = cmul(l32, l32);
    {
      const int rbase = seg * 4096 + (sch & 1) * 8, rch = (4 * tg + (sch >> 1)) ^ ((seg & 3) << 2);
      f32x2 v[16]; f32x4 st[8];
#pragma unroll
      for (int t = 0; t < 16; t++) v[t] = *(LAS f32x2*)(L + rbase + t * 256 + ((rch ^ (t & 7)) << 4));
#pragma unroll
      for (int t = 0; t < 8; t++) st[t] = *(LAS f32x4*)(L + M_STT + (16 * seg + 2 * t) * 8);
#pragma unroll
      for (int t = 0; t < 16; t++) {
        const float mu = st[t >> 1][(t & 1) * 2], rs = st[t >> 1][(t & 1) * 2 + 1];
        bu[t] = {mul_(fnma_(mu, c2.x, v[t].x), rs), mul_(fnma_(mu, c2.y, v[t].y), rs)};
        h = cfma(lam, h, bu[t]);
      }
      { cplx o = {__shfl_up(h.r, 8), __shfl_up(h.i, 8)}; if (seg >= 1) h = cfma(l16, o, h); }
      { cplx o = {__shfl_up(h.r, 16), __shfl_up(h.i, 16)}; if (seg >= 2) h = cfma(l32, o, h); }
      { cplx o = {__shfl_up(h.r, 32), __shfl_up(h.i, 32)}; if (seg >= 4) h = cfma(l64, o, h); }
      if (seg == 7 && r < 15) {
        const unsigned ur = __builtin_bit_cast(unsigned, h.r), ui = __builtin_bit_cast(unsigned, h.i);
        GAS u64* gp = (GAS u64*)(ws + WS_E + ((size_t)r * 512 + chg) * 16);
        __hip_atomic_store(gp, ((u64)((unsigned)tag + hashA(ur, ui)) << 32) | ur, RLX_AGENT);
        __hip_atomic_store(gp + 1, ((u64)((unsigned)(tag >> 32) + hashB(ur, ui)) << 32) | ui, RLX_AGENT);
      }
      { cplx o = {__shfl_up(h.r, 8), __shfl_up(h.i, 8)}; if (seg >= 1) init0 = o; }
    }
    ST(6);
    lds_arrive(CNT + 5);
    issueB(1); issueB(2);
    cplx carry = {0.f, 0.f};
    if (r > 0) {
      const bool needA = seg < r, needB = seg + 8 < r;
      GAS u64* ga = (GAS u64*)(ws + WS_E + ((size_t)seg * 512 + chg) * 16);
      GAS u64* gb = ga + (size_t)8 * 512 * 2;
      u64 a0 = 0, a1 = 0, b0 = 0, b1 = 0;
#pragma nounroll
      for (unsigned spins = 0; spins < SPIN_CAP; ++spins) {
        bool ok = true;
        if (needA) { a0 = __hip_atomic_load(ga, RLX_AGENT); a1 = __hip_atomic_load(ga + 1, RLX_AGENT); }
        if (needB) { b0 = __hip_atomic_load(gb, RLX_AGENT); b1 = __hip_atomic_load(gb + 1, RLX_AGENT); }
        if (needA) ok = (unsigned)(a0 >> 32) == (unsigned)tag + hashA((unsigned)a0, (unsigned)a1) &&
                        (unsigned)(a1 >> 32) == (unsigned)(tag >> 32) + hashB((unsigned)a0, (unsigned)a1);
        if (needB) ok = ok && (unsigned)(b0 >> 32) == (unsigned)tag + hashA((unsigned)b0, (unsigned)b1) &&
                        (unsigned)(b1 >> 32) == (unsigned)(tag >> 32) + hashB((unsigned)b0, (unsigned)b1);
        if (__all(ok)) break;
        __builtin_amdgcn_s_sleep(2);
      }
      const cplx ea = {__builtin_bit_cast(float, (unsigned)a0), __builtin_bit_cast(float, (unsigned)a1)};
      const cplx eb = {__builtin_bit_cast(float, (unsigned)b0), __builtin_bit_cast(float, (unsigned)b1)};
      cplx s1 = {0.f, 0.f};
      if (needA) s1 = cmul(wa, ea);
      if (needB) s1 = cfma(wb, eb, s1);
      s1.r = add_(s1.r, __shfl_xor(s1.r, 8)); s1.i = add_(s1.i, __shfl_xor(s1.i, 8));
      s1.r = add_(s1.r, __shfl_xor(s1.r, 16)); s1.i = add_(s1.i, __shfl_xor(s1.i, 16));
      s1.r = add_(s1.r, __shfl_xor(s1.r, 32)); s1.i = add_(s1.i, __shfl_xor(s1.i, 32));
      carry = s1;
    }
    ST(7);
    h = cfma(lseg, carry, init0);
    LAS char* tr = L + M_TR + tg * 4224;
#pragma unroll
    for (int t = 0; t < 16; t++) {
      h = cfma(lam, h, bu[t]);
      const cplx o = cmul(coef, h);
      const unsigned pk = pkbf(o.r, -o.i);
      LAS u16* dst = (LAS u16*)(tr + seg * 528 + t * 32 + sch * 2);
      dst[0] = (u16)pk; dst[8] = (u16)(pk >> 16);
    }
    CBAR();
#pragma unroll
    for (int j = 0; j < 4; j++) {
      const int part = j & 1, token = (j >> 1) * 64 + lane;
      const v4u v = *(LAS v4u*)(tr + (token >> 4) * 528 + (token & 15) * 32 + part * 16);
      const int T16 = 8 * r + (token >> 4);
      __builtin_amdgcn_raw_buffer_store_b128(v, rsw, (int)WS_H + (((T16 >> 2) * KS2 + 56 + 2 * c + part) * 4 + (T16 & 3)) * 1024 + ((token & 15) + 16 * tg) * 16, 0, AUX_SC1);
    }
    waitv<0>();
    if (lds_arrive(CNT + 4) == 3u && lane == 0) __hip_atomic_store((GAS u64*)(ws + WS_FL + r * 256 + 128 + c * 8), tag, RLX_AGENT);
    __builtin_amdgcn_s_setprio(0);
    lds_wait(CNT + 5, 4u);
    issueB(3); issueB(4);
  }
  ST(8);
  v2u ex[4], ed; unsigned es[4][2];
  if (FFN) {
#pragma unroll
    for (int j = 0; j < 4; j++) ex[j] = *(const v2u*)(a.x + (size_t)(64 * rt + 16 * j + (tid >> 4)) * D_ + 64 * ct + 4 * (tid & 15));
    ed = *(const v2u*)(a.Dsk + 64 * ct + 4 * (tid & 15));
  }
  if (FFN) {
    GAS u64* f = (GAS u64*)(ws + WS_FL + (rt >> 1) * 256) + (lane < 4 ? 4 * tg + lane : 0);
#pragma nounroll
    for (unsigned spins = 0; spins < SPIN_CAP; ++spins) {
      if (__all(__hip_atomic_load(f, RLX_AGENT) == tag)) break;
      __builtin_amdgcn_s_sleep(PSLEEP);
    }
    __builtin_amdgcn_fence(__ATOMIC_ACQUIRE, "wavefront");
  }
  else poll_flags(ws + WS_FL + (rt >> 1) * 256 + 128 + tg * 32, 4, tag);
  ST(9);
  const char* ap = ws + WS_H + ((size_t)rt * KS2 + s0) * 4096;
  if (FFN) {
#pragma unroll
    for (int j = 0; j < 4; j++) {
      const v2u t = __builtin_amdgcn_raw_buffer_load_b64(rsw, (int)WS_ST + (64 * rt + 16 * j + (tid >> 4)) * 8, 0, AUX_SC1);
      es[j][0] = t.x; es[j][1] = t.y;
    }
  }
  v4u ra[WA + 1][4];
#pragma unroll
  for (int i = 0; i < WA; i++) aload4(ra[i], lane * 16, ap + (size_t)i * 4096);
  if (FFN) lds_wait(CNT + 0, 4u);
  const int fw = (2 * g + (q >> 1)) & 3;
  unsigned toff[4];
#pragma unroll
  for (int j = 0; j < 4; j++) toff[j] = FFN ? (4 * g + q) * 128 + ((j ^ fw) << 5) + p4 * 8 : tok * 64 + ((g ^ ((-(tok >> 2)) & 3)) << 4) + j * 1024;
  f32x4 acc2[4][4];
#pragma unroll
  for (int i = 0; i < 4; i++)
#pragma unroll
    for (int j = 0; j < 4; j++) acc2[i][j] = f32x4{0, 0, 0, 0};
  W8 wf[2];
  auto rdw = [&](int i, int j) {
    const unsigned ad = (unsigned)(size_t)ringp(i) + toff[j];
    if (FFN) tr2k(wf[i & 1].h[2 * j], wf[i & 1].h[2 * j + 1], ad); else rd2h(wf[i & 1].h[2 * j], wf[i & 1].h[2 * j + 1], ad);
  };
#pragma unroll
  for (int j = 0; j < 4; j++) rdw(0, j);
  waitl_pin8(wf[0]);
  static_for<N2>([&](auto ic) {
    constexpr int i = decltype(ic)::value;
    waitv<p2_wait(i, N2, WA, RD)>();
    pin4(ra[i % (WA + 1)]);
    if (i == 0) ST(10);
    if (i == 4) ST(11);
    SB0();
    static_for<16>([&](auto mc) {
      constexpr int m = decltype(mc)::value, j = m >> 2, t = m & 3;
      const v4u wv = {wf[i & 1].h[2 * j].x, wf[i & 1].h[2 * j].y, wf[i & 1].h[2 * j + 1].x, wf[i & 1].h[2 * j + 1].y};
      acc2[j][t] = mfma(wv, ra[i % (WA + 1)][t], acc2[j][t]);
      SB0();
      if constexpr (m >= RDM && m < RDM + 4 && i + 1 < N2) rdw(i + 1, m - RDM);
      if constexpr (m >= GLM && m < GLM + 4 && i + RD < N2) glds16(FFN ? srcF(tg, i + RD, m - GLM) : srcS(tg, i + RD, m - GLM), ringp(i + RD) + (m - GLM) * 1024);
      if constexpr (m >= 8 && m < 12 && i + WA < N2) aload1<(m - 8) * 1024>(ra[(i + WA) % (WA + 1)][m - 8], lane * 16, ap + (size_t)(i + WA) * 4096);
      SB0();
    });
    keep8(wf[i & 1]);
    if constexpr (i + 1 < N2) waitl_pin8(wf[(i + 1) & 1]);
  });
  ST(12);
  auto pieceF = [&](int f, int t) { return L + (t < 3 ? 65536 + f * 12288 + t * 4096 : M_TR + f * 4096); };
  auto pieceS = [&](int f, int t) { return L + (t == 0 ? 114688 + f * 4096 : t < 3 ? 32768 + f * 8192 + (t - 1) * 4096 : f * 8192); };
  if (FFN) lds_wait(CNT + 4, 4u);
#pragma unroll
  for (int t = 0; t < 4; t++)
#pragma unroll
    for (int j = 0; j < 4; j++)
      *(LAS f32x4*)((FFN ? pieceF(tg, t) : pieceS(tg, t)) + (j * 4 + g) * 256 + ((tok + 4 * j + g) & 15) * 16) = acc2[j][t];
  lds_arrive(CNT + (FFN ? 8 : 9));
  if (!FFN) {
    if (b == 255 && w == 7) {
      poll_flags(ws + WS_FL + 15 * 256 + 128, 16, tag);
      if (lane == 0) __hip_atomic_store((GAS u64*)(ws + WS_EP), tag, RLX_AGENT);
    }
  } else {
    const __amdgpu_buffer_rsrc_t rso = __builtin_amdgcn_make_buffer_rsrc((void*)a.out, 0, 2048 * D_ * 4, 0x00020000);
    const int po = (tid & 15) * 256 + (((tid >> 4) + (tid & 15)) & 15) * 16;
    f32x4 sk[4], gg[4];
#pragma unroll
    for (int j = 0; j < 4; j++)
#pragma unroll
      for (int e = 0; e < 4; e++) {
        const unsigned xw = e < 2 ? ex[j].x : ex[j].y, dw = e < 2 ? ed.x : ed.y;
        const float xv = (e & 1) ? bfhi(xw) : bf2f(xw), dv = (e & 1) ? bfhi(dw) : bf2f(dw);
        sk[j][e] = rbf(mul_(rbf(fdiv(rbf(add_(xv, -__builtin_bit_cast(float, es[j][0]))), __builtin_bit_cast(float, es[j][1]))), dv));
      }
    lds_wait(CNT + 8, 4u);
#pragma unroll
    for (int j = 0; j < 4; j++) {
      const f32x4 p0 = *(LAS f32x4*)(pieceF(0, j) + po), p1 = *(LAS f32x4*)(pieceF(1, j) + po), p2 = *(LAS f32x4*)(pieceF(2, j) + po), p3 = *(LAS f32x4*)(pieceF(3, j) + po);
#pragma unroll
      for (int e = 0; e < 4; e++) gg[j][e] = rbf(silu_f(rbf(add_(add_(p0[e], p1[e]), add_(p2[e], p3[e])))));
    }
    lds_wait(CNT + 9, 4u);
    ST(13);
#pragma unroll
    for (int j = 0; j < 4; j++) {
      const f32x4 sy = (*(LAS f32x4*)(pieceS(0, j) + po) + *(LAS f32x4*)(pieceS(1, j) + po)) + (*(LAS f32x4*)(pieceS(2, j) + po) + *(LAS f32x4*)(pieceS(3, j) + po));
      const f32x4 xv = {bf2f(ex[j].x), bfhi(ex[j].x), bf2f(ex[j].y), bfhi(ex[j].y)};
      __builtin_amdgcn_raw_buffer_store_b128(__builtin_bit_cast(v4u, xv + (2.f * sy + sk[j]) * gg[j]), rso, ((64 * rt + 16 * j + (tid >> 4)) * D_ + 64 * ct + 4 * (tid & 15)) * 4, 0, 16);
    }
  }
  ST(14);
  ST_FLUSH(ws);
}

__global__ __launch_bounds__(512, 2) void k_fused(Args a) {
  extern __shared__ __attribute__((aligned(16))) char smem[];
  LAS char* L = (LAS char*)smem;
  const int w = __builtin_amdgcn_readfirstlane(threadIdx.x >> 6);
  if (w < 4) body<true>(a, L, w);
  else body<false>(a, L, w);
}
}

extern "C" void kernel_launch(void* const* d_in, const int* in_sizes, int n_in, void* d_out, int out_size, void* d_ws, size_t ws_size,
                              hipStream_t stream) {
  Args a;
  a.x = (const u16*)d_in[0]; a.Lre = (const u16*)d_in[1]; a.Lim = (const u16*)d_in[2];
  a.Bre = (const u16*)d_in[3]; a.Bim = (const u16*)d_in[4]; a.Cre = (const u16*)d_in[5]; a.Cim = (const u16*)d_in[6];
  a.logdt = (const u16*)d_in[7]; a.Dsk = (const u16*)d_in[8]; a.w1 = (const u16*)d_in[10]; a.w2 = (const u16*)d_in[11];
  a.ws = (char*)d_ws; a.out = (float*)d_out;
  (void)hipFuncSetAttribute((const void*)k_fused, hipFuncAttributeMaxDynamicSharedMemorySize, LDS_BYTES);
  k_fused<<<256, 512, LDS_BYTES, stream>>>(a);
}
```

```cpp
#include <hip/hip_runtime.h>
#include <hip/hip_bf16.h>
#include <stdint.h>
#include <utility>

typedef unsigned short u16;
typedef unsigned long long u64;
typedef unsigned v4u __attribute__((ext_vector_type(4)));
typedef unsigned v2u __attribute__((ext_vector_type(2)));
typedef float f32x4 __attribute__((ext_vector_type(4)));
typedef float f32x2 __attribute__((ext_vector_type(2)));
typedef short bf16x8 __attribute__((ext_vector_type(8)));
typedef short v4s __attribute__((ext_vector_type(4)));
typedef __bf16 v2bf __attribute__((ext_vector_type(2)));
#define LAS __attribute__((address_space(3)))
#define GAS __attribute__((address_space(1)))
#define RLX_AGENT __ATOMIC_RELAXED, __HIP_MEMORY_SCOPE_AGENT
#define RLX_WG __ATOMIC_RELAXED, __HIP_MEMORY_SCOPE_WORKGROUP

namespace {
constexpr int D_ = 512, F_ = 1792;
constexpr int KS2 = 88;
constexpr size_t WS_H = 0;
constexpr size_t WS_H_BYTES = (size_t)32 * KS2 * 4096;
constexpr size_t WS_ST = WS_H + WS_H_BYTES;
constexpr size_t WS_E = WS_ST + 2048 * 8;
constexpr size_t WS_FL = WS_E + 16 * 512 * 16;
constexpr size_t WS_EP = WS_FL + 16 * 256;
constexpr size_t WS_END = WS_EP + 128;
constexpr int SLOT = 32768, OFF_W = 16384;
constexpr int M_STT = 131072, M_CS = M_STT + 1024, M_PRM = M_CS + 512, M_ST2 = M_PRM + 512, M_CNT = M_ST2 + 512, M_TR = M_CNT + 256;
constexpr int LDS_BYTES = 163840;
constexpr unsigned CODE_BYTES = 36864;
constexpr unsigned SPIN_CAP = 1u << 17;
#define FAUX 16
constexpr int AUX_SC1 = 16;
#define RDM 0
#define GLM 4
#define WSLEEP 4
#define W2AT 1
#define PSLEEP 6

__device__ __forceinline__ float bf2f(unsigned b) { return __builtin_bit_cast(float, b << 16); }
__device__ __forceinline__ float bfhi(unsigned b) { return __builtin_bit_cast(float, b & 0xffff0000u); }
__device__ __forceinline__ unsigned pkbf(float lo, float hi) {
  v2bf v; v.x = (__bf16)lo; v.y = (__bf16)hi; return __builtin_bit_cast(unsigned, v);
}
__device__ __forceinline__ float rbf(float f) { return bf2f(pkbf(f, 0.f)); }
__device__ __forceinline__ f32x4 mfma(v4u a, v4u b, f32x4 c) {
  return __builtin_amdgcn_mfma_f32_16x16x32_bf16(__builtin_bit_cast(bf16x8, a), __builtin_bit_cast(bf16x8, b), c, 0, 0, 0);
}
__device__ __forceinline__ float dot2(unsigned a, unsigned b, float c) {
  return __builtin_amdgcn_fdot2_f32_bf16(__builtin_bit_cast(v2bf, a), __builtin_bit_cast(v2bf, b), c, false);
}
__device__ __forceinline__ void glds16(const void* g, LAS char* l) {
  __builtin_amdgcn_global_load_lds((const GAS void*)g, (LAS void*)l, 16, 0, 0);
}
__device__ __forceinline__ void touch(const void* g, LAS char* scratch) { __builtin_amdgcn_global_load_lds((const GAS void*)g, (LAS void*)scratch, 4, 0, 0); }
__device__ __forceinline__ v2u trread(LAS char* p) {
  return __builtin_bit_cast(v2u, __builtin_amdgcn_ds_read_tr16_b64_v4i16((LAS v4s*)p));
}
__device__ __forceinline__ unsigned silu2(unsigned hb) {
  const f32x2 h = {bf2f(hb), bfhi(hb)};
  const f32x2 t = h * -1.44269504088896f;
  const f32x2 d = f32x2{__builtin_amdgcn_exp2f(t.x), __builtin_amdgcn_exp2f(t.y)} + 1.f;
  const f32x2 y = h * f32x2{__builtin_amdgcn_rcpf(d.x), __builtin_amdgcn_rcpf(d.y)};
  return pkbf(y.x, y.y);
}
struct cplx { float r, i; };
__device__ __forceinline__ float fma_(float a, float b, float c) { float d; asm("v_fma_f32 %0, %1, %2, %3" : "=v"(d) : "v"(a), "v"(b), "v"(c)); return d; }
__device__ __forceinline__ float fnma_(float a, float b, float c) { float d; asm("v_fma_f32 %0, -%1, %2, %3" : "=v"(d) : "v"(a), "v"(b), "v"(c)); return d; }
__device__ __forceinline__ float mul_(float a, float b) { float d; asm("v_mul_f32 %0, %1, %2" : "=v"(d) : "v"(a), "v"(b)); return d; }
__device__ __forceinline__ float add_(float a, float b) { float d; asm("v_add_f32 %0, %1, %2" : "=v"(d) : "v"(a), "v"(b)); return d; }
__device__ __forceinline__ float mul_t(float a, float b) { float d; asm("s_nop 0\n\tv_mul_f32 %0, %1, %2" : "=v"(d) : "v"(a), "v"(b)); return d; }
__device__ __forceinline__ float add_t(float a, float b) { float d; asm("s_nop 0\n\tv_add_f32 %0, %1, %2" : "=v"(d) : "v"(a), "v"(b)); return d; }
__device__ __forceinline__ float silu_f(float h) { return mul_t(h, __builtin_amdgcn_rcpf(add_t(1.f, __builtin_amdgcn_exp2f(mul_(h, -1.44269504088896f))))); }
__device__ __forceinline__ cplx cmul(cplx a, cplx b) { return {fnma_(a.i, b.i, mul_(a.r, b.r)), fma_(a.r, b.i, mul_(a.i, b.r))}; }
__device__ __forceinline__ cplx cfma(cplx a, cplx b, cplx c) {
  return {fma_(a.r, b.r, fnma_(a.i, b.i, c.r)), fma_(a.r, b.i, fma_(a.i, b.r, c.i))};
}
__device__ __forceinline__ unsigned hashA(unsigned a, unsigned b) { unsigned h = a * 0x9E3779B1u + (b ^ (b >> 15)) * 0x85EBCA77u; return h ^ (h >> 13); }
__device__ __forceinline__ unsigned hashB(unsigned a, unsigned b) { unsigned h = b * 0xC2B2AE3Du + (a ^ (a >> 16)) * 0x27D4EB2Fu; return h ^ (h >> 15); }
template <int N> __device__ __forceinline__ void waitv() { asm volatile("s_waitcnt vmcnt(%0)" ::"n"(N) : "memory"); }
#define WAITL0() __builtin_amdgcn_s_waitcnt(0xC07F)
#define BAR() __builtin_amdgcn_s_barrier()
#define SB0() __builtin_amdgcn_sched_barrier(0)
#define CBAR() asm volatile("" ::: "memory")
#define ST_DECL
#define ST(i)
#define SF(i)
#define ST_FLUSH(wsp)

__device__ __forceinline__ unsigned lds_arrive(LAS unsigned* c) {
  unsigned o = 0;
  if ((threadIdx.x & 63) == 0)
    asm volatile("s_waitcnt lgkmcnt(0)\n\tds_add_rtn_u32 %0, %1, %2\n\ts_waitcnt lgkmcnt(0)" : "=&v"(o) : "v"((unsigned)(size_t)c), "v"(1u) : "memory");
  return __builtin_amdgcn_readfirstlane(o);
}
__device__ __forceinline__ void lds_wait(LAS unsigned* c, unsigned want) {
#pragma nounroll
  for (unsigned spins = 0; spins < (1u << 22); ++spins) {
    unsigned v;
    asm volatile("ds_read_b32 %0, %1\n\ts_waitcnt lgkmcnt(0)" : "=&v"(v) : "v"((unsigned)(size_t)c) : "memory");
    if (__builtin_amdgcn_readfirstlane(v) >= want) break;
    __builtin_amdgcn_s_sleep(1);
  }
}
struct Tr14 { v2u h[14]; };
template <int OFF> __device__ __forceinline__ void tr14(Tr14& t, const unsigned (&ad)[7]) {
  asm volatile("ds_read_b64_tr_b16 %0, %14 offset:%21\n\tds_read_b64_tr_b16 %1, %14 offset:%22\n\t"
               "ds_read_b64_tr_b16 %2, %15 offset:%21\n\tds_read_b64_tr_b16 %3, %15 offset:%22\n\t"
               "ds_read_b64_tr_b16 %4, %16 offset:%21\n\tds_read_b64_tr_b16 %5, %16 offset:%22\n\t"
               "ds_read_b64_tr_b16 %6, %17 offset:%21\n\tds_read_b64_tr_b16 %7, %17 offset:%22\n\t"
               "ds_read_b64_tr_b16 %8, %18 offset:%21\n\tds_read_b64_tr_b16 %9, %18 offset:%22\n\t"
               "ds_read_b64_tr_b16 %10, %19 offset:%21\n\tds_read_b64_tr_b16 %11, %19 offset:%22\n\t"
               "ds_read_b64_tr_b16 %12, %20 offset:%21\n\tds_read_b64_tr_b16 %13, %20 offset:%22"
               : "=&v"(t.h[0]), "=&v"(t.h[1]), "=&v"(t.h[2]), "=&v"(t.h[3]), "=&v"(t.h[4]), "=&v"(t.h[5]), "=&v"(t.h[6]),
                 "=&v"(t.h[7]), "=&v"(t.h[8]), "=&v"(t.h[9]), "=&v"(t.h[10]), "=&v"(t.h[11]), "=&v"(t.h[12]), "=&v"(t.h[13])
               : "v"(ad[0]), "v"(ad[1]), "v"(ad[2]), "v"(ad[3]), "v"(ad[4]), "v"(ad[5]), "v"(ad[6]), "n"(OFF), "n"(OFF + 1024)
               : "memory");
}
template <int OFF> __device__ __forceinline__ void rd2x(v4u (&x)[2], unsigned ad) {
  asm volatile("ds_read_b128 %0, %2 offset:%3\n\tds_read_b128 %1, %2 offset:%4" : "=&v"(x[0]), "=&v"(x[1]) : "v"(ad), "n"(OFF), "n"(OFF + 2048) : "memory");
}
template <int N> __device__ __forceinline__ void waitl_pin(Tr14& t, v4u (&x)[2]) {
  asm volatile("s_waitcnt lgkmcnt(%16)"
               : "+v"(t.h[0]), "+v"(t.h[1]), "+v"(t.h[2]), "+v"(t.h[3]), "+v"(t.h[4]), "+v"(t.h[5]), "+v"(t.h[6]), "+v"(t.h[7]), "+v"(t.h[8]),
                 "+v"(t.h[9]), "+v"(t.h[10]), "+v"(t.h[11]), "+v"(t.h[12]), "+v"(t.h[13]), "+v"(x[0]), "+v"(x[1])
               : "n"(N) : "memory");
}
template <int OFF> __device__ __forceinline__ void tr2(v2u& h0, v2u& h1, unsigned ad) {
  asm volatile("ds_read_b64_tr_b16 %0, %2 offset:%3\n\tds_read_b64_tr_b16 %1, %2 offset:%4" : "=&v"(h0), "=&v"(h1) : "v"(ad), "n"(OFF), "n"(OFF + 1024) : "memory");
}
__device__ __forceinline__ void keep(const Tr14& t, const v4u (&x)[2]) {
  asm volatile("" :: "v"(t.h[0]), "v"(t.h[1]), "v"(t.h[2]), "v"(t.h[3]), "v"(t.h[4]), "v"(t.h[5]), "v"(t.h[6]), "v"(t.h[7]), "v"(t.h[8]),
               "v"(t.h[9]), "v"(t.h[10]), "v"(t.h[11]), "v"(t.h[12]), "v"(t.h[13]), "v"(x[0]), "v"(x[1]));
}
__device__ __forceinline__ void waitl_pinS(v4u (&x)[2], v2u (&t)[4]) {
  asm volatile("s_waitcnt lgkmcnt(0)" : "+v"(x[0]), "+v"(x[1]), "+v"(t[0]), "+v"(t[1]), "+v"(t[2]), "+v"(t[3]) :: "memory");
}
template <int OFF> __device__ __forceinline__ void tr4(v2u (&h)[4], unsigned a0, unsigned a1) {
  asm volatile("ds_read_b64_tr_b16 %0, %4 offset:%6\n\tds_read_b64_tr_b16 %1, %4 offset:%7\n\t"
               "ds_read_b64_tr_b16 %2, %5 offset:%6\n\tds_read_b64_tr_b16 %3, %5 offset:%7"
               : "=&v"(h[0]), "=&v"(h[1]), "=&v"(h[2]), "=&v"(h[3]) : "v"(a0), "v"(a1), "n"(OFF), "n"(OFF + 1024) : "memory");
}
template <int OFF> __device__ __forceinline__ void rd4w(v4u (&x)[4], unsigned ad) {
  asm volatile("ds_read_b128 %0, %4 offset:%5\n\tds_read_b128 %1, %4 offset:%6\n\tds_read_b128 %2, %4 offset:%7\n\tds_read_b128 %3, %4 offset:%8"
               : "=&v"(x[0]), "=&v"(x[1]), "=&v"(x[2]), "=&v"(x[3]) : "v"(ad), "n"(OFF), "n"(OFF + 2048), "n"(OFF + 4096), "n"(OFF + 6144) : "memory");
}
template <int OFF> __device__ __forceinline__ void rd1(v4u& x, unsigned ad) {
  asm volatile("ds_read_b128 %0, %1 offset:%2" : "=&v"(x) : "v"(ad), "n"(OFF) : "memory");
}
template <int N> __device__ __forceinline__ void waitl_pin5(v4u (&x)[4], v4u& y) {
  asm volatile("s_waitcnt lgkmcnt(%5)" : "+v"(x[0]), "+v"(x[1]), "+v"(x[2]), "+v"(x[3]), "+v"(y) : "n"(N) : "memory");
}
template <int N> __device__ __forceinline__ void waitl_pin4(v4u (&x)[4]) {
  asm volatile("s_waitcnt lgkmcnt(%4)" : "+v"(x[0]), "+v"(x[1]), "+v"(x[2]), "+v"(x[3]) : "n"(N) : "memory");
}
__device__ __forceinline__ float fdiv(float a, float b) {
  const float rc = __builtin_amdgcn_rcpf(b), q0 = mul_t(a, rc);
  return fma_(fnma_(q0, b, a), rc, q0);
}
typedef std::integral_constant<int, 0> I0;
#define IC(n) std::integral_constant<int, (n)>{}
#define CAS __attribute__((address_space(4)))
__device__ __forceinline__ void wload(unsigned& d, const char* p) { asm volatile("global_load_dword %0, %1, off" : "=&v"(d) : "v"(p) : "memory"); }
struct W8 { v2u h[8]; };
__device__ __forceinline__ void tr2k(v2u& h0, v2u& h1, unsigned ad) {
  asm volatile("ds_read_b64_tr_b16 %0, %2\n\tds_read_b64_tr_b16 %1, %2 offset:2048" : "=&v"(h0), "=&v"(h1) : "v"(ad) : "memory");
}
__device__ __forceinline__ void rd2h(v2u& h0, v2u& h1, unsigned ad) {
  asm volatile("ds_read_b64 %0, %2\n\tds_read_b64 %1, %2 offset:8" : "=&v"(h0), "=&v"(h1) : "v"(ad) : "memory");
}
__device__ __forceinline__ void waitl_pin8(W8& t) {
  asm volatile("s_waitcnt lgkmcnt(0)" : "+v"(t.h[0]), "+v"(t.h[1]), "+v"(t.h[2]), "+v"(t.h[3]), "+v"(t.h[4]), "+v"(t.h[5]), "+v"(t.h[6]), "+v"(t.h[7]) :: "memory");
}
__device__ __forceinline__ void keep8(const W8& t) {
  asm volatile("" :: "v"(t.h[0]), "v"(t.h[1]), "v"(t.h[2]), "v"(t.h[3]), "v"(t.h[4]), "v"(t.h[5]), "v"(t.h[6]), "v"(t.h[7]));
}
__device__ __forceinline__ void pin4(v4u (&x)[4]) { asm volatile("" : "+v"(x[0]), "+v"(x[1]), "+v"(x[2]), "+v"(x[3])); }
template <int OFF> __device__ __forceinline__ void aload1(v4u& r, unsigned voff, const char* sbase) {
  asm volatile("global_load_dwordx4 %0, %1, %2 offset:%3 sc1" : "=&v"(r) : "v"(voff), "s"(sbase), "n"(OFF) : "memory");
}
__device__ __forceinline__ void poll_flags(const char* p, int n, u64 tag) {
  const int lane = threadIdx.x & 63;
  GAS u64* f = (GAS u64*)p + (lane < n ? lane : 0);
#pragma nounroll
  for (unsigned spins = 0; spins < SPIN_CAP; ++spins) {
    if (__all(__hip_atomic_load(f, RLX_AGENT) == tag)) break;
    __builtin_amdgcn_s_sleep(PSLEEP);
  }
  __builtin_amdgcn_fence(__ATOMIC_ACQUIRE, "wavefront");
}
__device__ __forceinline__ void aload4(v4u (&r)[4], unsigned voff, const char* sbase) {
  asm volatile("global_load_dwordx4 %0, %4, %5 sc1\n\tglobal_load_dwordx4 %1, %4, %5 offset:1024 sc1\n\t"
               "global_load_dwordx4 %2, %4, %5 offset:2048 sc1\n\tglobal_load_dwordx4 %3, %4, %5 offset:3072 sc1"
               : "=&v"(r[0]), "=&v"(r[1]), "=&v"(r[2]), "=&v"(r[3]) : "v"(voff), "s"(sbase) : "memory");
}
__device__ __forceinline__ void trread8(v4u (&wf)[4], unsigned a0, unsigned a1, unsigned a2, unsigned a3) {
  v2u x0, x1, x2, x3, x4, x5, x6, x7;
  asm volatile("ds_read_b64_tr_b16 %0, %8\n\tds_read_b64_tr_b16 %1, %8 offset:2048\n\t"
               "ds_read_b64_tr_b16 %2, %9\n\tds_read_b64_tr_b16 %3, %9 offset:2048\n\t"
               "ds_read_b64_tr_b16 %4, %10\n\tds_read_b64_tr_b16 %5, %10 offset:2048\n\t"
               "ds_read_b64_tr_b16 %6, %11\n\tds_read_b64_tr_b16 %7, %11 offset:2048\n\ts_waitcnt lgkmcnt(0)"
               : "=&v"(x0), "=&v"(x1), "=&v"(x2), "=&v"(x3), "=&v"(x4), "=&v"(x5), "=&v"(x6), "=&v"(x7)
               : "v"(a0), "v"(a1), "v"(a2), "v"(a3) : "memory");
  wf[0] = v4u{x0.x, x0.y, x1.x, x1.y}; wf[1] = v4u{x2.x, x2.y, x3.x, x3.y};
  wf[2] = v4u{x4.x, x4.y, x5.x, x5.y}; wf[3] = v4u{x6.x, x6.y, x7.x, x7.y};
}
constexpr int p2_wait(int i, int N, int W, int RD) {
  int total = 0, need = 0;
  for (int j = 0; j < W && j < N; j++) { total += 4; if (j == i) need = total; }
  for (int s = 0; s < i; s++) {
    if (s + RD < N) { total += 4; if (s + RD == i + 1) need = total; }
    if (s + W < N) { total += 4; if (s + W == i) need = total > need ? total : need; }
  }
  return total - need;
}

template <int... I, class Fn> __device__ __forceinline__ void static_for_(std::integer_sequence<int, I...>, Fn&& f) { (f(std::integral_constant<int, I>{}), ...); }
template <int N, class Fn> __device__ __forceinline__ void static_for(Fn&& f) { static_for_(std::make_integer_sequence<int, N>{}, f); }

struct Args {
  const u16 *x, *Lre, *Lim, *Bre, *Bim, *Cre, *Cim, *logdt, *Dsk, *w1, *w2;
  char* ws; float* out;
};

template <bool FFN>
__device__ __forceinline__ void body(const Args& a, LAS char* L, const int w) {
  const int b = blockIdx.x, xcd = b & 7, idx = b >> 3;
  const int r = (xcd & 1) * 8 + (idx & 7), c = (xcd >> 1) * 4 + (idx >> 3);
  const int rt = 4 * xcd + (idx & 3), ct = idx >> 2;
  const int tid = threadIdx.x, lane = tid & 63, tok = lane & 15, g = lane >> 4, tg = w & 3;
  const int codd = c & 1;
  char* const ws = a.ws;
  LAS unsigned* CNT = (LAS unsigned*)(L + M_CNT);
  ST_DECL ST(0);
  const __amdgpu_buffer_rsrc_t rsw = __builtin_amdgcn_make_buffer_rsrc((void*)ws, 0, (int)WS_END, 0x00020000);
  const int r8 = lane >> 3, p8 = lane & 7;
  constexpr int N2 = FFN ? 14 : 8;
  const int s0 = FFN ? 14 * tg : 56 + 8 * tg;
  const char* bw0 = (const char*)(a.w2 + 64 * ct);
  const unsigned bwl = (r8 * D_ + (((((p8 >> 1) ^ ((r8 >> 1) & 3)) << 1) | (p8 & 1)) << 3)) * 2;
  const int r4 = lane >> 2, c4 = lane & 3;
  const size_t bc0 = (size_t)(64 * ct) * 1024;
  const unsigned bcl = (r4 * 512 + ((c4 ^ ((-(r4 >> 2)) & 3)) << 3)) * 2;
  constexpr int RD = FFN ? 3 : 5, WA = FFN ? 4 : 7;
  auto ringp = [&](int i) {
    const int e = i % RD;
    return L + (FFN ? 65536 + tg * 12288 + e * 4096 : e == 0 ? 114688 + tg * 4096 : e < 3 ? 32768 + tg * 8192 + (e - 1) * 4096 : tg * 8192 + (e - 3) * 4096);
  };
  auto srcF = [&](int f, int i, int j) { return bw0 + (size_t)(32 * (14 * f + i) + 8 * j) * (D_ * 2) + bwl; };
  auto srcS = [&](int f, int i, int j) { return (const char*)((i & 1) ? a.Cim : a.Cre) + bc0 + 256 * f + 64 * (i >> 1) + (size_t)j * 16 * 1024 + bcl; };
  auto issueB = [&](int i) {
    LAS char* sl = ringp(i);
#pragma unroll
    for (int j = 0; j < 4; j++) glds16(FFN ? srcF(tg, i, j) : srcS(tg, i, j), sl + j * 1024);
  };
  const int swz = (tok >> 1) & 7;
  const int xoff = (32 * tg + tok) * 128 + ((g ^ swz) << 4);
  const int q = tok >> 2, p4 = lane & 3, fl = q | ((g & 1) << 2);
  const unsigned lb = (unsigned)(size_t)L;
  const unsigned xad[4] = {lb + xoff, lb + (xoff ^ 64), lb + xoff + 2 * SLOT, lb + (xoff ^ 64) + 2 * SLOT};
  auto waddr = [&](int v) { const int pi = codd ? (v == 6 ? 1 : v + 2) : v; return lb + OFF_W + (8 * g + q) * 256 + ((pi ^ fl) << 5) + p4 * 8; };
  constexpr unsigned ONES = 0x3f803f80u;
  LAS f32x2* STT = (LAS f32x2*)(L + M_STT);
  LAS float* CS = (LAS float*)(L + M_CS);
  u64 tag;

  if (FFN) {
    tag = *(const CAS u64*)(ws + WS_EP) + 1ull;
    if (tid < 16) CNT[tid] = 0u;
    LAS char* junk = L + M_TR + 16896 + tg * 256;
    if (w == 0 && lane < 32) {
      const float lr = bf2f(a.Lre[32 * c + lane]), li = bf2f(a.Lim[32 * c + lane]), dt = rbf(expf(bf2f(a.logdt[32 * c + lane])));
      const float er = expf(dt * lr); float sn, cn; sincosf(dt * li, &sn, &cn);
      const float br = er * cn, bi = er * sn, den = 1.f / (lr * lr + li * li), nr = br - 1.f;
      *(LAS f32x4*)(L + M_PRM + lane * 16) = f32x4{br, bi, (nr * lr + bi * li) * den, (bi * lr - nr * li) * den};
    }
    if (tid < 152) {
      const int t = b * 152 + tid;
      const char* p = t < 14336 ? (const char*)a.w1 + (size_t)t * 128
                    : t < 30720 ? (const char*)a.x + (size_t)(t - 14336) * 128
                    : t < 34816 ? (const char*)a.Bre + (size_t)(t - 30720) * 128 : (const char*)a.Bim + (size_t)(t - 34816) * 128;
      __builtin_amdgcn_s_sleep(WSLEEP);
      touch(p, junk);
    }
    const int t2 = b * 88 + (tid < 88 ? tid : 0);
    const char* wp2 = t2 < 14336 ? (const char*)a.w2 + (size_t)t2 * 128
                    : t2 < 18432 ? (const char*)a.Cre + (size_t)(t2 - 14336) * 128 : (const char*)a.Cim + (size_t)(t2 - 18432) * 128;
    unsigned wlo[7], whi[7];
#pragma unroll
    for (int v = 0; v < 7; v++) { wlo[v] = waddr(v); whi[v] = wlo[v] + 2 * SLOT; }
    f32x4 acc[7][2];
#pragma unroll
    for (int u = 0; u < 7; u++) { acc[u][0] = f32x4{0, 0, 0, 0}; acc[u][1] = f32x4{0, 0, 0, 0}; }
    v4u xf[2][2]; Tr14 wt[2];
    auto rd = [&](auto kc) {
      constexpr int ks = decltype(kc)::value, kk = ks & 1, sl = (ks >> 1) & 3;
      rd2x<(sl & 1) * SLOT>(xf[kk], xad[(sl >> 1) * 2 + kk]);
      tr14<(sl & 1) * SLOT + kk * 8192>(wt[kk], (sl >> 1) ? whi : wlo);
    };
    auto step = [&](auto kc) {
      constexpr int ks = decltype(kc)::value, kk = ks & 1, kn = ks + 1, nk = kn & 1, sl = (kn >> 1) & 3;
      static_for<14>([&](auto mc) {
        constexpr int m = decltype(mc)::value, v = m >> 1, tt = m & 1;
        const v4u wv = {wt[kk].h[2 * v].x, wt[kk].h[2 * v].y, wt[kk].h[2 * v + 1].x, wt[kk].h[2 * v + 1].y};
        acc[v][tt] = mfma(wv, xf[kk][tt], acc[v][tt]);
        SB0();
        if constexpr (kn < 16) {
          if (m == 0) rd2x<(sl & 1) * SLOT>(xf[nk], xad[(sl >> 1) * 2 + nk]);
          if constexpr (m >= 1 && m <= 7) tr2<(sl & 1) * SLOT + nk * 8192>(wt[nk].h[2 * (m - 1)], wt[nk].h[2 * (m - 1) + 1], (sl >> 1) ? whi[m - 1] : wlo[m - 1]);
          SB0();
        }
      });
      keep(wt[kk], xf[kk]);
      if constexpr (kn < 16) waitl_pin<0>(wt[nk], xf[nk]);
    };
    WAITL0(); BAR();
    ST(1);
    rd(IC(0));
    waitl_pin<0>(wt[0], xf[0]);
    static_for<8>([&](auto sc) {
      constexpr int s = decltype(sc)::value;
      SF(1);
      step(IC(2 * s));
      SF(5);
      BAR();
      SF(6);
      if (s == 3) ST(2);
      if (s == W2AT && tg < 2) touch(wp2, junk);
      step(IC(2 * s + 1));
      SF(8);
    });
    ST(3);
    BAR();
    ST(4);
    {
      __builtin_amdgcn_s_setprio(3);
      const f32x2 st0 = STT[32 * tg + tok], st1 = STT[32 * tg + 16 + tok];
      f32x4 cs[7];
#pragma unroll
      for (int v = 0; v < 7; v++) cs[v] = *(LAS f32x4*)(CS + v * 16 + 4 * g);
      const int T16 = 8 * r + 2 * tg;
      const int fb = (int)WS_H + ((T16 >> 2) * KS2 * 4 + (T16 & 3)) * 1024 + lane * 16;
      const float km[2] = {-st0.x * st0.y, -st1.x * st1.y}, rs[2] = {st0.y, st1.y};
      v2u o[2][2];
#pragma unroll
      for (int v = 0; v < 7; v++) {
#pragma unroll
        for (int tt = 0; tt < 2; tt++) {
          unsigned pk[2];
#pragma unroll
          for (int hh = 0; hh < 2; hh++) {
            const f32x2 ac = {acc[v][tt][2 * hh], acc[v][tt][2 * hh + 1]}, cc = {cs[v][2 * hh], cs[v][2 * hh + 1]};
            const f32x2 u = __builtin_elementwise_fma(ac, f32x2{rs[tt], rs[tt]}, cc * km[tt]);
            unsigned hb = pkbf(u.x, u.y);
            asm volatile("" : "+v"(hb));
            pk[hh] = silu2(hb);
          }
          o[v & 1][tt] = v2u{pk[0], pk[1]};
        }
        if (v & 1) {
          const int U = 7 * c + v - 1 + codd;
#pragma unroll
          for (int tt = 0; tt < 2; tt++)
            __builtin_amdgcn_raw_buffer_store_b128(v4u{o[0][tt].x, o[0][tt].y, o[1][tt].x, o[1][tt].y}, rsw, fb + tt * 1024 + (U >> 1) * 4096, 0, FAUX);
        } else if (v == 6) {
          const int U = codd ? 7 * c : 7 * c + 6;
#pragma unroll
          for (int tt = 0; tt < 2; tt++)
            __builtin_amdgcn_raw_buffer_store_b64(o[0][tt], rsw, fb + tt * 1024 + (U >> 1) * 4096 + (U & 1) * 8, 0, FAUX);
        }
      }
    }
    ST(5);
    lds_arrive(CNT + 7);
    waitv<0>();
    if (lds_arrive(CNT + 3) == 3u) {
      lds_wait(CNT + 0, 4u);
      if (lane == 0) __hip_atomic_store((GAS u64*)(ws + WS_FL + r * 256 + c * 8), tag, RLX_AGENT);
    }
    ST(6);
    __builtin_amdgcn_s_setprio(0);
  } else {
    unsigned sx[2], sw[2];
#pragma unroll
    for (int h = 0; h < 2; h++) {
      sx[h] = ((8 * h + r8) * D_ + ((p8 ^ ((4 * h + (r8 >> 1)) & 7)) << 3)) * 2;
      sw[h] = ((8 * h + g) * F_ + (((((tok >> 1) ^ (g | (h << 2))) << 1) | (tok & 1)) << 3)) * 2;
    }
    const char* xb = (const char*)(a.x + (size_t)(128 * r + 32 * tg) * D_);
    const char* wb1 = (const char*)(a.w1 + (size_t)(16 * tg) * F_ + 112 * c - 16 * codd);
    const int rot = idx & 7;
    auto issue1 = [&](int s, int j) {
      LAS char* sb = L + (s & 3) * SLOT;
      const int k0 = ((s + rot) & 7) * 64;
      if (j < 4) glds16(xb + ((j >> 1) * 16 * D_ + k0) * 2 + sx[j & 1], sb + (4 * tg + j) * 1024);
      else glds16(wb1 + (size_t)(k0 + (j & 1) * 4) * F_ * 2 + sw[(j - 4) >> 1], sb + OFF_W + (4 * tg + j - 4) * 1024);
    };
    auto issue = [&](int s) {
#pragma unroll
      for (int j = 0; j < 8; j++) issue1(s, j);
    };
    issue(0); issue(1);
    __builtin_amdgcn_s_setprio(3);
    tag = *(const CAS u64*)(ws + WS_EP) + 1ull;
    const u16* ssp[2];
#pragma unroll
    for (int h = 0; h < 2; h++) {
      const int n = 16 * tg + 8 * h + r8;
      ssp[h] = ((n & 1) ? a.Bim : a.Bre) + (size_t)(32 * c + (n >> 1)) * D_ + ((p8 ^ ((n >> 1) & 7)) << 3);
    }
    auto issueS1 = [&](int js, int j) { glds16(ssp[j & 1] + ((4 * js + (j >> 1) + rot) & 7) * 64, L + js * SLOT + (j >> 1) * 8192 + (2 * tg + (j & 1)) * 1024); };
    auto issueR1 = [&](int i, int j) {
      const int o = 32 * i + 8 * tg + j;
      if (o < 48) glds16(srcF(o / 12, (o % 12) >> 2, o & 3), L + 65536 + o * 1024);
      else glds16(srcS((o - 48) >> 2, 0, o & 3), L + 65536 + o * 1024);
    };
    auto piece = [&](int s, int j) {
      if (s <= 3) issue1(s + 4, j); else if (s <= 5) issueS1(s - 4, j); else issueR1(s - 6, j);
    };
    const unsigned wa0 = waddr(tg), wa1 = waddr(tg == 3 ? 3 : tg + 4);
    const v4u ones = {ONES, ONES, ONES, ONES};
    v4u xr[16][2];
    v2u wq[2][4];
    f32x4 sm[2] = {{0, 0, 0, 0}, {0, 0, 0, 0}}, sq[2] = {{0, 0, 0, 0}, {0, 0, 0, 0}}, cf[2] = {{0, 0, 0, 0}, {0, 0, 0, 0}};
    auto rdS = [&](auto kc) {
      constexpr int ks = decltype(kc)::value, kk = ks & 1, sl = (ks >> 1) & 3;
      rd2x<(sl & 1) * SLOT>(xr[ks], xad[(sl >> 1) * 2 + kk]);
      tr4<(sl & 1) * SLOT + kk * 8192>(wq[kk], wa0 + (sl >> 1) * 2 * SLOT, wa1 + (sl >> 1) * 2 * SLOT);
    };
    auto pinS = [&](auto kc) {
      constexpr int ks = decltype(kc)::value, kk = ks & 1;
      waitl_pinS(xr[ks], wq[kk]);
    };
    auto stat = [&](auto kc, int s, int j0) {
      constexpr int ks = decltype(kc)::value, kk = ks & 1;
      sm[0] = mfma(ones, xr[ks][0], sm[0]); sq[0] = mfma(xr[ks][0], xr[ks][0], sq[0]);
      SB0(); if (j0 >= 0) piece(s, j0); SB0();
      sm[1] = mfma(ones, xr[ks][1], sm[1]);
      SB0(); if (j0 >= 0) piece(s, j0 + 1); SB0();
      sq[1] = mfma(xr[ks][1], xr[ks][1], sq[1]);
      SB0(); if (j0 >= 0) piece(s, j0 + 2); SB0();
      cf[0] = mfma(v4u{wq[kk][0].x, wq[kk][0].y, wq[kk][1].x, wq[kk][1].y}, ones, cf[0]);
      SB0(); if (j0 >= 0) piece(s, j0 + 3); SB0();
      cf[1] = mfma(v4u{wq[kk][2].x, wq[kk][2].y, wq[kk][3].x, wq[kk][3].y}, ones, cf[1]);
      SB0();
    };
    const int sch = lane & 7, seg = lane >> 3, chg = 32 * c + 8 * tg + sch;
    cplx lam, coef, l16, l32, l64, lseg = {1.f, 0.f}, wa = {0.f, 0.f}, wb = {0.f, 0.f};
    waitv<8>();
    BAR();
    ST(1);
    rdS(IC(0));
    issue(2);
    pinS(IC(0));
    static_for<8>([&](auto sc) {
      constexpr int s = decltype(sc)::value;
      SF(1);
      rdS(IC(2 * s + 1));
      if (s == 0) { stat(IC(0), -1, 0); issue1(3, 4); issue1(3, 5); issue1(3, 6); issue1(3, 7); }
      else stat(IC(2 * s), s - 1, 4);
      pinS(IC(2 * s + 1));
      SF(2);
      if (s <= 6) waitv<16>();
      SF(3);
      BAR();
      SF(6);
      if (s == 3) ST(2);
      if constexpr (s < 7) rdS(IC(2 * s + 2));
      stat(IC(2 * s + 1), s, 0);
      if constexpr (s < 7) pinS(IC(2 * s + 2));
      SF(8);
      if (s == 1) {
        const f32x4 pr = *(LAS f32x4*)(L + M_PRM + (8 * tg + sch) * 16);
        lam = {pr[0], pr[1]}; coef = {pr[2], pr[3]};
        const cplx l2 = cmul(lam, lam), l4 = cmul(l2, l2), l8 = cmul(l4, l4);
        l16 = cmul(l8, l8); l32 = cmul(l16, l16); l64 = cmul(l32, l32);
        const cplx l128 = cmul(l64, l64), l256 = cmul(l128, l128), l512 = cmul(l256, l256), l1024 = cmul(l512, l512);
        if (seg & 1) lseg = l16;
        if (seg & 2) lseg = cmul(lseg, l32);
        if (seg & 4) lseg = cmul(lseg, l64);
        const int nb = r - 1 - (seg + 8);
        cplx t = {1.f, 0.f};
        const int ne = nb >= 0 ? nb : (nb + 8 >= 0 ? nb + 8 : 0);
        if (ne & 1) t = l128;
        if (ne & 2) t = cmul(t, l256);
        if (ne & 4) t = cmul(t, l512);
        if (nb >= 0) { wb = t; wa = cmul(t, l1024); } else if (nb + 8 >= 0) { wa = t; }
        asm volatile("" : "+v"(wa.r), "+v"(wa.i), "+v"(wb.r), "+v"(wb.i), "+v"(lseg.r), "+v"(lseg.i));
      }
    });
    {
      const int e = tok & 3;
      const float d0 = e == 0 ? sq[0][0] : e == 1 ? sq[0][1] : e == 2 ? sq[0][2] : sq[0][3];
      const float d1 = e == 0 ? sq[1][0] : e == 1 ? sq[1][1] : e == 2 ? sq[1][2] : sq[1][3];
      const bool own = g == q;
#pragma unroll
      for (int tt = 0; tt < 2; tt++) {
        float m = sm[tt][0] * (1.f / 512.f);
        asm volatile("" : "+v"(m));
        const float var = fmaxf(fnma_(m, m, mul_(tt ? d1 : d0, 1.f / 512.f)), 0.f);
        const float mb = rbf(m), sd = rbf(__builtin_amdgcn_sqrtf(rbf(rbf(var) + 1e-5f)));
        if (own) {
          STT[32 * tg + 16 * tt + tok] = f32x2{mb, fdiv(1.f, sd)};
          if (c == 0)
            __builtin_amdgcn_raw_buffer_store_b64(v2u{__builtin_bit_cast(unsigned, mb), __builtin_bit_cast(unsigned, sd)}, rsw,
                                                  (int)WS_ST + (128 * r + 32 * tg + 16 * tt + tok) * 8, 0, AUX_SC1);
        }
      }
      if (tok == 0) {
        *(LAS f32x4*)(CS + tg * 16 + 4 * g) = cf[0];
        if (tg < 3) *(LAS f32x4*)(CS + (tg + 4) * 16 + 4 * g) = cf[1];
      }
    }
    ST(3);
    WAITL0(); BAR();
    ST(4);
#pragma unroll
    for (int j = 4; j < 8; j++) piece(7, j);
    __builtin_amdgcn_s_setprio(0);
    f32x4 acc[4][2];
    f32x4 cs4 = {0, 0, 0, 0};
#pragma unroll
    for (int u = 0; u < 4; u++) { acc[u][0] = f32x4{0, 0, 0, 0}; acc[u][1] = f32x4{0, 0, 0, 0}; }
    const unsigned sad[2] = {lb + tok * 128 + ((g ^ swz) << 4), lb + ((tok * 128 + ((g ^ swz) << 4)) ^ 64)};
    v4u wf[3][4], wo[3];
    const unsigned oad[2] = {sad[0] + tg * 2048, sad[1] + tg * 2048};
    waitv<0>();
    lds_arrive(CNT + 0); lds_wait(CNT + 0, 4u);
    rd4w<0>(wf[0], sad[0]); rd1<0>(wo[0], oad[0]);
    rd4w<0>(wf[1], sad[1]); rd1<0>(wo[1], oad[1]);
    lds_wait(CNT + 7, 4u);
    static_for<16>([&](auto kc) {
      constexpr int ks = decltype(kc)::value, kn = ks + 2;
      if constexpr (kn < 16) {
        rd4w<(kn >> 3) * SLOT + ((kn >> 1) & 3) * 8192>(wf[kn % 3], sad[kn & 1]);
        rd1<(kn >> 3) * SLOT + ((kn >> 1) & 3) * 8192>(wo[kn % 3], oad[kn & 1]);
        waitl_pin5<10>(wf[ks % 3], wo[ks % 3]);
      } else if constexpr (kn == 16) waitl_pin5<5>(wf[ks % 3], wo[ks % 3]);
      else waitl_pin5<0>(wf[ks % 3], wo[ks % 3]);
      SB0();
#pragma unroll
      for (int u = 0; u < 4; u++) { acc[u][0] = mfma(wf[ks % 3][u], xr[ks][0], acc[u][0]); acc[u][1] = mfma(wf[ks % 3][u], xr[ks][1], acc[u][1]); }
      cs4 = mfma(wo[ks % 3], ones, cs4);
      if (ks == 8) lds_arrive(CNT + 1);
      SB0();
    });
    ST(5);
    __builtin_amdgcn_s_setprio(3);
    lds_wait(CNT + 1, 4u);
#pragma unroll
    for (int u = 0; u < 4; u++)
#pragma unroll
      for (int tt = 0; tt < 2; tt++) {
        const int token = 32 * tg + 16 * tt + tok;
        *(LAS f32x4*)(L + token * 256 + (((4 * u + g) ^ (token & 7) ^ (((token >> 4) & 3) << 2)) << 4)) = acc[u][tt];
      }
    const float ca = lane & 1 ? cs4[2] : cs4[0], cb = lane & 1 ? cs4[3] : cs4[1];
    const f32x2 c2 = {__shfl(ca, (sch >> 1) * 16 + (sch & 1)), __shfl(cb, (sch >> 1) * 16 + (sch & 1))};
    lds_arrive(CNT + 2);
    lds_wait(CNT + 2, 4u);
    cplx bu[16], h = {0.f, 0.f}, init0 = {0.f, 0.f};
    asm volatile("" : "+v"(l16.r), "+v"(l16.i));
    l32 = cmul(l16, l16); l64 = cmul(l32, l32);
    {
      const int rbase = seg * 4096 + (sch & 1) * 8, rch = (4 * tg + (sch >> 1)) ^ ((seg & 3) << 2);
      f32x2 v[16]; f32x4 st[8];
#pragma unroll
      for (int t = 0; t < 16; t++) v[t] = *(LAS f32x2*)(L + rbase + t * 256 + ((rch ^ (t & 7)) << 4));
#pragma unroll
      for (int t = 0; t < 8; t++) st[t] = *(LAS f32x4*)(L + M_STT + (16 * seg + 2 * t) * 8);
#pragma unroll
      for (int t = 0; t < 16; t++) {
        const float mu = st[t >> 1][(t & 1) * 2], rs = st[t >> 1][(t & 1) * 2 + 1];
        bu[t] = {mul_(fnma_(mu, c2.x, v[t].x), rs), mul_(fnma_(mu, c2.y, v[t].y), rs)};
        h = cfma(lam, h, bu[t]);
      }
      { cplx o = {__shfl_up(h.r, 8), __shfl_up(h.i, 8)}; if (seg >= 1) h = cfma(l16, o, h); }
      { cplx o = {__shfl_up(h.r, 16), __shfl_up(h.i, 16)}; if (seg >= 2) h = cfma(l32, o, h); }
      { cplx o = {__shfl_up(h.r, 32), __shfl_up(h.i, 32)}; if (seg >= 4) h = cfma(l64, o, h); }
      if (seg == 7 && r < 15) {
        const unsigned ur = __builtin_bit_cast(unsigned, h.r), ui = __builtin_bit_cast(unsigned, h.i);
        GAS u64* gp = (GAS u64*)(ws + WS_E + ((size_t)r * 512 + chg) * 16);
        __hip_atomic_store(gp, ((u64)((unsigned)tag + hashA(ur, ui)) << 32) | ur, RLX_AGENT);
        __hip_atomic_store(gp + 1, ((u64)((unsigned)(tag >> 32) + hashB(ur, ui)) << 32) | ui, RLX_AGENT);
      }
      { cplx o = {__shfl_up(h.r, 8), __shfl_up(h.i, 8)}; if (seg >= 1) init0 = o; }
    }
    ST(6);
    lds_arrive(CNT + 5);
    issueB(1); issueB(2);
    cplx carry = {0.f, 0.f};
    if (r > 0) {
      const bool needA = seg < r, needB = seg + 8 < r;
      GAS u64* ga = (GAS u64*)(ws + WS_E + ((size_t)seg * 512 + chg) * 16);
      GAS u64* gb = ga + (size_t)8 * 512 * 2;
      u64 a0 = 0, a1 = 0, b0 = 0, b1 = 0;
#pragma nounroll
      for (unsigned spins = 0; spins < SPIN_CAP; ++spins) {
        bool ok = true;
        if (needA) { a0 = __hip_atomic_load(ga, RLX_AGENT); a1 = __hip_atomic_load(ga + 1, RLX_AGENT); }
        if (needB) { b0 = __hip_atomic_load(gb, RLX_AGENT); b1 = __hip_atomic_load(gb + 1, RLX_AGENT); }
        if (needA) ok = (unsigned)(a0 >> 32) == (unsigned)tag + hashA((unsigned)a0, (unsigned)a1) &&
                        (unsigned)(a1 >> 32) == (unsigned)(tag >> 32) + hashB((unsigned)a0, (unsigned)a1);
        if (needB) ok = ok && (unsigned)(b0 >> 32) == (unsigned)tag + hashA((unsigned)b0, (unsigned)b1) &&
                        (unsigned)(b1 >> 32) == (unsigned)(tag >> 32) + hashB((unsigned)b0, (unsigned)b1);
        if (__all(ok)) break;
        __builtin_amdgcn_s_sleep(2);
      }
      const cplx ea = {__builtin_bit_cast(float, (unsigned)a0), __builtin_bit_cast(float, (unsigned)a1)};
      const cplx eb = {__builtin_bit_cast(float, (unsigned)b0), __builtin_bit_cast(float, (unsigned)b1)};
      cplx s1 = {0.f, 0.f};
      if (needA) s1 = cmul(wa, ea);
      if (needB) s1 = cfma(wb, eb, s1);
      s1.r = add_(s1.r, __shfl_xor(s1.r, 8)); s1.i = add_(s1.i, __shfl_xor(s1.i, 8));
      s1.r = add_(s1.r, __shfl_xor(s1.r, 16)); s1.i = add_(s1.i, __shfl_xor(s1.i, 16));
      s1.r = add_(s1.r, __shfl_xor(s1.r, 32)); s1.i = add_(s1.i, __shfl_xor(s1.i, 32));
      carry = s1;
    }
    ST(7);
    h = cfma(lseg, carry, init0);
    LAS char* tr = L + M_TR + tg * 4224;
#pragma unroll
    for (int t = 0; t < 16; t++) {
      h = cfma(lam, h, bu[t]);
      const cplx o = cmul(coef, h);
      const unsigned pk = pkbf(o.r, -o.i);
      LAS u16* dst = (LAS u16*)(tr + seg * 528 + t * 32 + sch * 2);
      dst[0] = (u16)pk; dst[8] = (u16)(pk >> 16);
    }
    CBAR();
#pragma unroll
    for (int j = 0; j < 4; j++) {
      const int part = j & 1, token = (j >> 1) * 64 + lane;
      const v4u v = *(LAS v4u*)(tr + (token >> 4) * 528 + (token & 15) * 32 + part * 16);
      const int T16 = 8 * r + (token >> 4);
      __builtin_amdgcn_raw_buffer_store_b128(v, rsw, (int)WS_H + (((T16 >> 2) * KS2 + 56 + 2 * c + part) * 4 + (T16 & 3)) * 1024 + ((token & 15) + 16 * tg) * 16, 0, AUX_SC1);
    }
    waitv<0>();
    if (lds_arrive(CNT + 4) == 3u && lane == 0) __hip_atomic_store((GAS u64*)(ws + WS_FL + r * 256 + 128 + c * 8), tag, RLX_AGENT);
    __builtin_amdgcn_s_setprio(0);
    lds_wait(CNT + 5, 4u);
    issueB(3); issueB(4);
  }
  ST(8);
  v2u ex[4], ed; unsigned es[4][2];
  if (FFN) {
#pragma unroll
    for (int j = 0; j < 4; j++) ex[j] = *(const v2u*)(a.x + (size_t)(64 * rt + 16 * j + (tid >> 4)) * D_ + 64 * ct + 4 * (tid & 15));
    ed = *(const v2u*)(a.Dsk + 64 * ct + 4 * (tid & 15));
  }
  if (FFN) {
    GAS u64* f = (GAS u64*)(ws + WS_FL + (rt >> 1) * 256) + (lane < 4 ? 4 * tg + lane : 0);
#pragma nounroll
    for (unsigned spins = 0; spins < SPIN_CAP; ++spins) {
      if (__all(__hip_atomic_load(f, RLX_AGENT) == tag)) break;
      __builtin_amdgcn_s_sleep(PSLEEP);
    }
    __builtin_amdgcn_fence(__ATOMIC_ACQUIRE, "wavefront");
  }
  else poll_flags(ws + WS_FL + (rt >> 1) * 256 + 128 + tg * 32, 4, tag);
  ST(9);
  const char* ap = ws + WS_H + ((size_t)rt * KS2 + s0) * 4096;
  if (FFN) {
#pragma unroll
    for (int j = 0; j < 4; j++) {
      const v2u t = __builtin_amdgcn_raw_buffer_load_b64(rsw, (int)WS_ST + (64 * rt + 16 * j + (tid >> 4)) * 8, 0, AUX_SC1);
      es[j][0] = t.x; es[j][1] = t.y;
    }
  }
  v4u ra[WA + 1][4];
#pragma unroll
  for (int i = 0; i < WA; i++) aload4(ra[i], lane * 16, ap + (size_t)i * 4096);
  if (FFN) lds_wait(CNT + 0, 4u);
  const int fw = (2 * g + (q >> 1)) & 3;
  unsigned toff[4];
#pragma unroll
  for (int j = 0; j < 4; j++) toff[j] = FFN ? (4 * g + q) * 128 + ((j ^ fw) << 5) + p4 * 8 : tok * 64 + ((g ^ ((-(tok >> 2)) & 3)) << 4) + j * 1024;
  f32x4 acc2[4][4];
#pragma unroll
  for (int i = 0; i < 4; i++)
#pragma unroll
    for (int j = 0; j < 4; j++) acc2[i][j] = f32x4{0, 0, 0, 0};
  W8 wf[2];
  auto rdw = [&](int i, int j) {
    const unsigned ad = (unsigned)(size_t)ringp(i) + toff[j];
    if (FFN) tr2k(wf[i & 1].h[2 * j], wf[i & 1].h[2 * j + 1], ad); else rd2h(wf[i & 1].h[2 * j], wf[i & 1].h[2 * j + 1], ad);
  };
#pragma unroll
  for (int j = 0; j < 4; j++) rdw(0, j);
  waitl_pin8(wf[0]);
  static_for<N2>([&](auto ic) {
    constexpr int i = decltype(ic)::value;
    waitv<p2_wait(i, N2, WA, RD)>();
    pin4(ra[i % (WA + 1)]);
    if (i == 0) ST(10);
    if (i == 4) ST(11);
    SB0();
    static_for<16>([&](auto mc) {
      constexpr int m = decltype(mc)::value, j = m >> 2, t = m & 3;
      const v4u wv = {wf[i & 1].h[2 * j].x, wf[i & 1].h[2 * j].y, wf[i & 1].h[2 * j + 1].x, wf[i & 1].h[2 * j + 1].y};
      acc2[j][t] = mfma(wv, ra[i % (WA + 1)][t], acc2[j][t]);
      SB0();
      if constexpr (m >= RDM && m < RDM + 4 && i + 1 < N2) rdw(i + 1, m - RDM);
      if constexpr (m >= GLM && m < GLM + 4 && i + RD < N2) glds16(FFN ? srcF(tg, i + RD, m - GLM) : srcS(tg, i + RD, m - GLM), ringp(i + RD) + (m - GLM) * 1024);
      if constexpr (m >= 8 && m < 12 && i + WA < N2) aload1<(m - 8) * 1024>(ra[(i + WA) % (WA + 1)][m - 8], lane * 16, ap + (size_t)(i + WA) * 4096);
      SB0();
    });
    keep8(wf[i & 1]);
    if constexpr (i + 1 < N2) waitl_pin8(wf[(i + 1) & 1]);
  });
  ST(12);
  auto pieceF = [&](int f, int t) { return L + (t < 3 ? 65536 + f * 12288 + t * 4096 : M_TR + f * 4096); };
  auto pieceS = [&](int f, int t) { return L + (t == 0 ? 114688 + f * 4096 : t < 3 ? 32768 + f * 8192 + (t - 1) * 4096 : f * 8192); };
  if (FFN) lds_wait(CNT + 4, 4u);
#pragma unroll
  for (int t = 0; t < 4; t++)
#pragma unroll
    for (int j = 0; j < 4; j++)
      *(LAS f32x4*)((FFN ? pieceF(tg, t) : pieceS(tg, t)) + (j * 4 + g) * 256 + ((tok + 4 * j + g) & 15) * 16) = acc2[j][t];
  lds_arrive(CNT + (FFN ? 8 : 9));
  if (!FFN) {
    if (b == 255 && w == 7) {
      poll_flags(ws + WS_FL + 15 * 256 + 128, 16, tag);
      if (lane == 0) __hip_atomic_store((GAS u64*)(ws + WS_EP), tag, RLX_AGENT);
    }
  } else {
    const __amdgpu_buffer_rsrc_t rso = __builtin_amdgcn_make_buffer_rsrc((void*)a.out, 0, 2048 * D_ * 4, 0x00020000);
    const int po = (tid & 15) * 256 + (((tid >> 4) + (tid & 15)) & 15) * 16;
    f32x4 sk[4], gg[4];
#pragma unroll
    for (int j = 0; j < 4; j++)
#pragma unroll
      for (int e = 0; e < 4; e++) {
        const unsigned xw = e < 2 ? ex[j].x : ex[j].y, dw = e < 2 ? ed.x : ed.y;
        const float xv = (e & 1) ? bfhi(xw) : bf2f(xw), dv = (e & 1) ? bfhi(dw) : bf2f(dw);
        sk[j][e] = rbf(mul_(rbf(fdiv(rbf(add_(xv, -__builtin_bit_cast(float, es[j][0]))), __builtin_bit_cast(float, es[j][1]))), dv));
      }
    lds_wait(CNT + 8, 4u);
#pragma unroll
    for (int j = 0; j < 4; j++) {
      const f32x4 p0 = *(LAS f32x4*)(pieceF(0, j) + po), p1 = *(LAS f32x4*)(pieceF(1, j) + po), p2 = *(LAS f32x4*)(pieceF(2, j) + po), p3 = *(LAS f32x4*)(pieceF(3, j) + po);
#pragma unroll
      for (int e = 0; e < 4; e++) gg[j][e] = rbf(silu_f(rbf(add_(add_(p0[e], p1[e]), add_(p2[e], p3[e])))));
    }
    lds_wait(CNT + 9, 4u);
    ST(13);
#pragma unroll
    for (int j = 0; j < 4; j++) {
      const f32x4 sy = (*(LAS f32x4*)(pieceS(0, j) + po) + *(LAS f32x4*)(pieceS(1, j) + po)) + (*(LAS f32x4*)(pieceS(2, j) + po) + *(LAS f32x4*)(pieceS(3, j) + po));
      const f32x4 xv = {bf2f(ex[j].x), bfhi(ex[j].x), bf2f(ex[j].y), bfhi(ex[j].y)};
      __builtin_amdgcn_raw_buffer_store_b128(__builtin_bit_cast(v4u, xv + (2.f * sy + sk[j]) * gg[j]), rso, ((64 * rt + 16 * j + (tid >> 4)) * D_ + 64 * ct + 4 * (tid & 15)) * 4, 0, 16);
    }
  }
  ST(14);
  ST_FLUSH(ws);
}

__global__ __launch_bounds__(512, 2) void k_fused(Args a) {
  extern __shared__ __attribute__((aligned(16))) char smem[];
  LAS char* L = (LAS char*)smem;
  const int w = __builtin_amdgcn_readfirstlane(threadIdx.x >> 6);
  u64 pc; asm volatile("s_getpc_b64 %0" : "=s"(pc));
  if (w >= 1 && w < 4) {
    const char* pb = (const char*)(pc & ~127ull);
    const unsigned o0 = (threadIdx.x - 64u) * 128u;
    touch(pb + o0, L + M_TR + 16896 + w * 256);
    touch(pb + (o0 + 24576u < CODE_BYTES ? o0 + 24576u : 0u), L + M_TR + 16896 + w * 256);
  }
  if (w < 4) body<true>(a, L, w);
  else body<false>(a, L, w);
}
}

extern "C" void kernel_launch(void* const* d_in, const int* in_sizes, int n_in, void* d_out, int out_size, void* d_ws, size_t ws_size,
                              hipStream_t stream) {
  Args a;
  a.x = (const u16*)d_in[0]; a.Lre = (const u16*)d_in[1]; a.Lim = (const u16*)d_in[2];
  a.Bre = (const u16*)d_in[3]; a.Bim = (const u16*)d_in[4]; a.Cre = (const u16*)d_in[5]; a.Cim = (const u16*)d_in[6];
  a.logdt = (const u16*)d_in[7]; a.Dsk = (const u16*)d_in[8]; a.w1 = (const u16*)d_in[10]; a.w2 = (const u16*)d_in[11];
  a.ws = (char*)d_ws; a.out = (float*)d_out;
  (void)hipFuncSetAttribute((const void*)k_fused, hipFuncAttributeMaxDynamicSharedMemorySize, LDS_BYTES);
  k_fused<<<256, 512, LDS_BYTES, stream>>>(a);
}
```
